```python
import torch

def _softmax(x, axis=-1):
    return torch.softmax(x, dim=axis)
def _gelu(x):
    return torch.nn.functional.gelu(x, approximate="tanh")
def _silu(x):
    return torch.nn.functional.silu(x)
def _sigmoid(x):
    return torch.sigmoid(x)
def _layernorm(x, axis=-1, eps=1e-5):
    m = x.mean(dim=axis, keepdim=True)
    v = x.var(dim=axis, keepdim=True, unbiased=False)
    return (x - m) / torch.sqrt(v + eps)
def _scatter_add(tgt, idx, src, axis=0):
    return tgt.clone().index_add_(axis, idx, src)
def _segment_sum(src, seg, n):
    out = torch.zeros((n,) + src.shape[1:], dtype=src.dtype)
    return out.index_add_(0, seg, src)
def _pad(x, pw):
    flat = tuple(v for pair in reversed(pw) for v in pair)
    return torch.nn.functional.pad(x, flat)

kernel_name = 'interleave_layers__linear_kernel_hedgehog__dense_activation_squared_relu__prompt'

def setup_inputs():

    _w = (lambda a: (lambda t: t.to(torch.bfloat16) if t.is_floating_point() else t)(torch.as_tensor(a)))
    def _A_setup():
        B, S, D, H = 1, 2048, 576, 32
        Dh = D // H
        return {
            "x": _w((lambda *s: torch.randn(*s, dtype=torch.bfloat16) * 0.05)(B, S, D)),
            "wqkv": _w((lambda *s: torch.randn(*s, dtype=torch.bfloat16) * 0.2)(D, 3 * D)),
            "w_phi": _w((lambda *s: torch.randn(*s, dtype=torch.bfloat16) * 0.2)(H, Dh, Dh) * (Dh ** -0.5)),
            "wo": _w((lambda *s: torch.randn(*s, dtype=torch.bfloat16) * 0.1)(D, D)),
        }
    _A_in = _A_setup()

    def _B_setup():
        B, S, D, F = 1, 2048, 576, 4608
        return {
            "x": _w((lambda *s: torch.randn(*s, dtype=torch.bfloat16) * 0.05)(B, S, D)),
            "w1": _w((lambda *s: torch.randn(*s, dtype=torch.bfloat16) * 0.05)(D, F)),
            "b1": _w((lambda *s: torch.randn(*s, dtype=torch.bfloat16) * 0.2)(F)),
            "w2": _w((lambda *s: torch.randn(*s, dtype=torch.bfloat16) * 0.05)(F, D)),
            "b2": _w((lambda *s: torch.randn(*s, dtype=torch.bfloat16) * 1.6)(D)),
        }
    _B_in = _B_setup()

    return {
        "A_x": _A_in["x"],
        "A_wqkv": _A_in["wqkv"],
        "A_w_phi": _A_in["w_phi"],
        "A_wo": _A_in["wo"],
        "B_x": _B_in["x"],
        "B_w1": _B_in["w1"],
        "B_b1": _B_in["b1"],
        "B_w2": _B_in["w2"],
        "B_b2": _B_in["b2"],
    }

def reference(A_x, A_wqkv, A_w_phi, A_wo, B_x, B_w1, B_b1, B_w2, B_b2):
    def _A_ref(x, wqkv, w_phi, wo):
        B, S, D = x.shape
        H = 32; Dh = D // H
        qkv = (lambda x, *d: x.permute(*d))((x @ wqkv).reshape(B, S, 3, H, Dh), 2, 0, 3, 1, 4)
        q, k, v = qkv[0], qkv[1], qkv[2]
        pq = torch.einsum("bhsd,hdf->bhsf", q, w_phi)
        pk = torch.einsum("bhsd,hdf->bhsf", k, w_phi)
        phi_q = torch.concatenate([_softmax(pq, axis=-1), _softmax(-pq, axis=-1)], axis=-1)
        phi_k = torch.concatenate([_softmax(pk, axis=-1), _softmax(-pk, axis=-1)], axis=-1)
        kv = torch.einsum("bhsf,bhsd->bhfd", phi_k, v)
        z  = torch.einsum("bhsf->bhf", phi_k)
        num = torch.einsum("bhqf,bhfd->bhqd", phi_q, kv)
        den = torch.einsum("bhqf,bhf->bhq", phi_q, z)[..., None] + 1e-6
        o = num / den
        return (lambda x, *d: x.permute(*d))(o, 0, 2, 1, 3).reshape(B, S, D) @ wo

    def _B_ref(x, w1, b1, w2, b2):
        h = torch.einsum("bsd,df->bsf", x, w1) + b1
        zero = torch.zeros_like(h)
        r = torch.where(h > zero, h, zero)
        h = r * r
        out = torch.einsum("bsf,fd->bsd", h, w2) + b2
        return out

    x = A_x
    x = x + _A_ref(_layernorm(x, axis=-1), A_wqkv, A_w_phi, A_wo)
    x = x + _B_ref(_layernorm(x, axis=-1), B_w1, B_b1, B_w2, B_b2)
    x = x + _A_ref(_layernorm(x, axis=-1), A_wqkv, A_w_phi, A_wo)
    x = x + _B_ref(_layernorm(x, axis=-1), B_w1, B_b1, B_w2, B_b2)
    return x
```

```cpp
#include <hip/hip_runtime.h>
#include <hip/hip_bf16.h>
#include <stdint.h>

typedef __hip_bfloat16 bf16;
#define S_ 2048
#define D_ 576
#define H_ 32
#define DH_ 18
#define F_ 4608

__device__ __forceinline__ float b2f(bf16 x) { return __bfloat162float(x); }
__device__ __forceinline__ bf16 f2b(float x) { return __float2bfloat16(x); }
__device__ __forceinline__ float rb(float x) { return b2f(f2b(x)); }

__global__ void n_ln(const bf16* x, bf16* xn) {
  int row = blockIdx.x;
  __shared__ float red[256];
  float s = 0;
  for (int i = threadIdx.x; i < D_; i += 256) s += b2f(x[row * D_ + i]);
  red[threadIdx.x] = s; __syncthreads();
  for (int o = 128; o > 0; o >>= 1) { if (threadIdx.x < o) red[threadIdx.x] += red[threadIdx.x + o]; __syncthreads(); }
  float m = red[0] / D_; __syncthreads();
  float v = 0;
  for (int i = threadIdx.x; i < D_; i += 256) { float d = b2f(x[row * D_ + i]) - m; v += d * d; }
  red[threadIdx.x] = v; __syncthreads();
  for (int o = 128; o > 0; o >>= 1) { if (threadIdx.x < o) red[threadIdx.x] += red[threadIdx.x + o]; __syncthreads(); }
  float var = red[0] / D_;
  float r = 1.0f / sqrtf(var + 1e-5f);
  for (int i = threadIdx.x; i < D_; i += 256) xn[row * D_ + i] = f2b((b2f(x[row * D_ + i]) - m) * r);
}

__global__ void n_gemm(const bf16* A, const bf16* B, bf16* C, int M, int N, int K, const bf16* bias, const bf16* resid, int mode) {
  __shared__ float As[16][17], Bs[16][17];
  int tx = threadIdx.x, ty = threadIdx.y;
  int row = blockIdx.y * 16 + ty, col = blockIdx.x * 16 + tx;
  float acc = 0;
  for (int k0 = 0; k0 < K; k0 += 16) {
    As[ty][tx] = b2f(A[(size_t)row * K + k0 + tx]);
    Bs[ty][tx] = b2f(B[(size_t)(k0 + ty) * N + col]);
    __syncthreads();
    for (int k = 0; k < 16; ++k) acc += As[ty][k] * Bs[k][tx];
    __syncthreads();
  }
  float r = rb(acc);
  if (mode == 1) { r = rb(r + b2f(bias[col])); r = r > 0 ? r : 0; r = rb(r * r); }
  else if (mode == 2) { if (bias) r = rb(r + b2f(bias[col])); r = rb(r + b2f(resid[(size_t)row * N + col])); }
  C[(size_t)row * N + col] = f2b(r);
}

__global__ void n_phi(const bf16* qkv, const bf16* wphi, bf16* phiq, bf16* phik) {
  int idx = blockIdx.x * blockDim.x + threadIdx.x;
  if (idx >= S_ * H_ * 2) return;
  int which = idx & 1, h = (idx >> 1) % H_, s = idx / (2 * H_);
  float q[DH_], p[DH_];
  for (int d = 0; d < DH_; ++d) q[d] = b2f(qkv[(size_t)s * 3 * D_ + which * D_ + h * DH_ + d]);
  float mx = -1e30f, mn = 1e30f;
  for (int f = 0; f < DH_; ++f) {
    float a = 0;
    for (int d = 0; d < DH_; ++d) a += q[d] * b2f(wphi[(h * DH_ + d) * DH_ + f]);
    a = rb(a); p[f] = a; mx = fmaxf(mx, a); mn = fminf(mn, a);
  }
  float s1 = 0, s2 = 0;
  for (int f = 0; f < DH_; ++f) { s1 += expf(p[f] - mx); s2 += expf(mn - p[f]); }
  bf16* out = (which ? phik : phiq) + ((size_t)s * H_ + h) * 36;
  for (int f = 0; f < DH_; ++f) { out[f] = f2b(expf(p[f] - mx) / s1); out[DH_ + f] = f2b(expf(mn - p[f]) / s2); }
}

__global__ void n_kv(const bf16* phik, const bf16* qkv, float* kv) {
  int idx = blockIdx.x * blockDim.x + threadIdx.x;
  if (idx >= H_ * 36 * 19) return;
  int d = idx % 19, f = (idx / 19) % 36, h = idx / (19 * 36);
  float a = 0;
  for (int s = 0; s < S_; ++s) {
    float pk = b2f(phik[((size_t)s * H_ + h) * 36 + f]);
    float v = d < 18 ? b2f(qkv[(size_t)s * 3 * D_ + 2 * D_ + h * DH_ + d]) : 1.0f;
    a += pk * v;
  }
  kv[idx] = rb(a);
}

__global__ void n_o(const bf16* phiq, const float* kv, bf16* o) {
  int idx = blockIdx.x * blockDim.x + threadIdx.x;
  if (idx >= S_ * H_) return;
  int h = idx % H_, s = idx / H_;
  float pq[36];
  for (int f = 0; f < 36; ++f) pq[f] = b2f(phiq[((size_t)s * H_ + h) * 36 + f]);
  float den = 0;
  for (int f = 0; f < 36; ++f) den += pq[f] * kv[(h * 36 + f) * 19 + 18];
  den = rb(rb(den) + 1e-6f);
  for (int d = 0; d < DH_; ++d) {
    float a = 0;
    for (int f = 0; f < 36; ++f) a += pq[f] * kv[(h * 36 + f) * 19 + d];
    o[(size_t)s * D_ + h * DH_ + d] = f2b(rb(a) / den);
  }
}

extern "C" void kernel_launch(void* const* d_in, const int* in_sizes, int n_in,
                              void* d_out, int out_size, void* d_ws, size_t ws_size,
                              hipStream_t stream) {
  const bf16* x0 = (const bf16*)d_in[0];
  const bf16* wqkv = (const bf16*)d_in[1];
  const bf16* wphi = (const bf16*)d_in[2];
  const bf16* wo = (const bf16*)d_in[3];
  const bf16* w1 = (const bf16*)d_in[5];
  const bf16* b1 = (const bf16*)d_in[6];
  const bf16* w2 = (const bf16*)d_in[7];
  const bf16* b2 = (const bf16*)d_in[8];
  char* ws = (char*)d_ws;
  size_t off = 0;
  auto alloc = [&](size_t bytes) { void* p = ws + off; off += (bytes + 255) & ~(size_t)255; return p; };
  bf16* xn = (bf16*)alloc(S_ * D_ * 2);
  bf16* qkv = (bf16*)alloc(S_ * 3 * D_ * 2);
  bf16* phiq = (bf16*)alloc(S_ * H_ * 36 * 2);
  bf16* phik = (bf16*)alloc(S_ * H_ * 36 * 2);
  float* kv = (float*)alloc(H_ * 36 * 19 * 4);
  bf16* o = (bf16*)alloc(S_ * D_ * 2);
  bf16* x1 = (bf16*)alloc(S_ * D_ * 2);
  bf16* x2 = (bf16*)alloc(S_ * D_ * 2);
  bf16* h = (bf16*)alloc((size_t)S_ * F_ * 2);
  const bf16* xin = x0;
  for (int layer = 0; layer < 2; ++layer) {
    bf16* xout = layer == 0 ? x2 : (bf16*)d_out;
    n_ln<<<S_, 256, 0, stream>>>(xin, xn);
    n_gemm<<<dim3(3 * D_ / 16, S_ / 16), dim3(16, 16), 0, stream>>>(xn, wqkv, qkv, S_, 3 * D_, D_, nullptr, nullptr, 0);
    n_phi<<<(S_ * H_ * 2 + 255) / 256, 256, 0, stream>>>(qkv, wphi, phiq, phik);
    n_kv<<<(H_ * 36 * 19 + 63) / 64, 64, 0, stream>>>(phik, qkv, kv);
    n_o<<<(S_ * H_ + 255) / 256, 256, 0, stream>>>(phiq, kv, o);
    n_gemm<<<dim3(D_ / 16, S_ / 16), dim3(16, 16), 0, stream>>>(o, wo, x1, S_, D_, D_, nullptr, xin, 2);
    n_ln<<<S_, 256, 0, stream>>>(x1, xn);
    n_gemm<<<dim3(F_ / 16, S_ / 16), dim3(16, 16), 0, stream>>>(xn, w1, h, S_, F_, D_, b1, nullptr, 1);
    n_gemm<<<dim3(D_ / 16, S_ / 16), dim3(16, 16), 0, stream>>>(h, w2, xout, S_, D_, F_, b2, x1, 2);
    xin = xout;
  }
}
```

```cpp
#include <hip/hip_runtime.h>
#include <hip/hip_bf16.h>
#include <stdint.h>

typedef __hip_bfloat16 bf16;
typedef short s4 __attribute__((ext_vector_type(4)));
typedef short s8 __attribute__((ext_vector_type(8)));
typedef float f4 __attribute__((ext_vector_type(4)));
typedef __bf16 bf8 __attribute__((ext_vector_type(8)));
typedef unsigned u4 __attribute__((ext_vector_type(4)));
typedef unsigned u2 __attribute__((ext_vector_type(2)));
#define AS3 __attribute__((address_space(3)))
#define AS1 __attribute__((address_space(1)))
#define DI __device__ __forceinline__

#define S_ 2048
#define D_ 576
#define H_ 32
#define DH_ 18
#define F_ 4608

DI float b2f(bf16 x) { return __bfloat162float(x); }
DI bf16 f2b(float x) { return __float2bfloat16(x); }
DI float rb(float x) { return b2f(f2b(x)); }
DI float bfu(unsigned short u) { return __uint_as_float(((unsigned)u) << 16); }
DI unsigned pack2(float a, float b) {
  bf16 x = f2b(a), y = f2b(b);
  return (unsigned)__builtin_bit_cast(unsigned short, x) | ((unsigned)__builtin_bit_cast(unsigned short, y) << 16);
}
typedef float f2 __attribute__((ext_vector_type(2)));
typedef short s2 __attribute__((ext_vector_type(2)));
typedef __bf16 bf2 __attribute__((ext_vector_type(2)));
DI unsigned cvtpk(f2 v) { return __builtin_bit_cast(unsigned, __builtin_convertvector(v, bf2)); }
DI f2 unpk(unsigned u) { f2 r; r[0] = __uint_as_float(u << 16); r[1] = __uint_as_float(u & 0xffff0000u); return r; }
DI f4 mfma32(s8 a, s8 b, f4 c) { return __builtin_amdgcn_mfma_f32_16x16x32_bf16(__builtin_bit_cast(bf8, a), __builtin_bit_cast(bf8, b), c, 0, 0, 0); }
#define LDS_R128(d, addr, off) asm volatile("ds_read_b128 %0, %1 offset:%2" : "=v"(d) : "v"(addr), "i"(off))
#define LDS_RTR(d, addr, off) asm volatile("ds_read_b64_tr_b16 %0, %1 offset:%2" : "=v"(d) : "v"(addr), "i"(off))
DI unsigned lds_addr(const void* p) { return (unsigned)(uintptr_t)(AS3 const char*)p; }
DI void glds16(const void* g, char* l) { __builtin_amdgcn_global_load_lds((const AS1 unsigned*)g, (AS3 unsigned*)l, 16, 0, 0); }
#define WAIT_V(n) asm volatile("s_waitcnt vmcnt(" #n ")" ::: "memory")

DI size_t hF_index(int t, int f) {
  int ks = f >> 5, r = f & 31, hi = r >> 4, g = (r & 15) >> 2, e = (r & 3) | (hi << 2);
  return ((((size_t)(t >> 4) * (F_ / 32) + ks) * 64 + g * 16 + (t & 15)) << 3) + e;
}
__global__ void kPw2(const bf16* __restrict__ w2, bf16* __restrict__ w2F) {
  int idx = blockIdx.x * 256 + threadIdx.x;
  if (idx >= 8 * 72 * 9 * 64) return;
  int lane = idx & 63, u = (idx >> 6) % 9, kp = (idx / (64 * 9)) % 72, ct = idx / (64 * 9 * 72);
  int i = lane & 15, g = lane >> 4;
  int ks, n;
  if (u < 8) { ks = 2 * kp + (u >> 2); n = 72 * ct + 16 * (u & 3) + i; } else { ks = 2 * kp + (i >> 3); n = 72 * ct + 64 + (i & 7); }
  unsigned short v[8];
#pragma unroll
  for (int e = 0; e < 8; ++e) { int f = 32 * ks + (e < 4 ? 4 * g + e : 16 + 4 * g + e - 4); v[e] = __builtin_bit_cast(unsigned short, w2[(size_t)f * D_ + n]); }
  u4 o;
#pragma unroll
  for (int e = 0; e < 4; ++e) o[e] = (unsigned)v[2 * e] | ((unsigned)v[2 * e + 1] << 16);
  *(u4*)(w2F + (size_t)idx * 8) = o;
}

DI size_t xF_index(int t, int k) { return ((((size_t)(t >> 4) * 18 + (k >> 5)) * 64 + ((k >> 3) & 3) * 16 + (t & 15)) << 3) + (k & 7); }
__global__ void n_toF(const bf16* x, bf16* xF) {
  int idx = blockIdx.x * 256 + threadIdx.x;
  if (idx >= S_ * D_) return;
  xF[xF_index(idx / D_, idx % D_)] = x[idx];
}
__global__ void n_fromF(const bf16* xF, bf16* x) {
  int idx = blockIdx.x * 256 + threadIdx.x;
  if (idx >= S_ * D_) return;
  x[idx] = xF[xF_index(idx / D_, idx % D_)];
}
#define SB() __builtin_amdgcn_sched_barrier(0)
#define BAR0() asm volatile("s_barrier" ::: "memory")
#define BARRIER() asm volatile("s_waitcnt lgkmcnt(0)\n\ts_barrier" ::: "memory")

#define VAR 0
#define D_R 16
#define D_PD 8
template <bool FINAL>
__global__ __launch_bounds__(448, 2) void kD(const bf16* __restrict__ hF, const bf16* __restrict__ w2F, const bf16* __restrict__ b2,
                                             const bf16* __restrict__ xresF, bf16* __restrict__ xout, float* __restrict__ stats) {
  __shared__ __attribute__((aligned(16))) char smem[D_R * 9216];
  const int tid = threadIdx.x, lane = tid & 63, w = tid >> 6;
  int bid = blockIdx.x;
  bid = (bid & 7) * 32 + (bid >> 3);
  const int rt = bid >> 3, ct = bid & 7;
  constexpr int NU = 72;
  if (w >= 4) {
    if (VAR & 64) { BAR0(); return; }
    const int lw = w - 4;
    const char* src = (const char*)w2F + (size_t)((VAR & 2) ? 0 : ct) * NU * 9216 + lw * 3072 + lane * 16;
    char* dst0 = smem + lw * 3072;
#pragma unroll
    for (int u = 0; u < D_R - 2; ++u)
#pragma unroll
      for (int j = 0; j < 3; ++j) glds16(src + u * 9216 + j * 1024, dst0 + u * 9216 + j * 1024);
    for (int u = 0; u < NU - (D_R - 2); ++u) {
      asm volatile("s_waitcnt vmcnt(%0)\n\ts_barrier" :: "i"(3 * (D_R - 3)) : "memory");
      const int un = u + D_R - 2;
      if (!(VAR & 16)) {
        char* dst = dst0 + (un % D_R) * 9216;
#pragma unroll
        for (int j = 0; j < 3; ++j) glds16(src + un * 9216 + j * 1024, dst + j * 1024);
      }
    }
#pragma unroll
    for (int r = D_R - 3; r >= 0; --r) asm volatile("s_waitcnt vmcnt(%0)\n\ts_barrier" :: "i"(3 * r) : "memory");
    return;
  }
  const int fi = lane & 15, fg = lane >> 4;
  const s8* hp = (const s8*)hF + (size_t)(((VAR & 1) ? 0 : rt) * 4 + w) * 144 * 64 + lane;
  const AS3 s8* wl = (const AS3 s8*)smem + lane;
  f4 acc[6];
#pragma unroll
  for (int a = 0; a < 6; ++a) acc[a] = (f4){0, 0, 0, 0};
  s8 hb[D_PD][2];
#pragma unroll
  for (int p = 0; p < D_PD; ++p) { hb[p][0] = hp[(2 * p) * 64]; SB(); hb[p][1] = hp[(2 * p + 1) * 64]; SB(); }
  s8 wf[2][9];
  BAR0();
#pragma unroll
  for (int j = 0; j < 9; ++j) wf[0][j] = wl[j * 64];
  SB();
  auto body = [&](int u0, bool last) {
#pragma unroll
    for (int q = 0; q < 8; ++q) {
      const int u = u0 + q;
      const bool rd = !last || q < 7;
      if (rd && !(VAR & 64)) BAR0();
      const AS3 s8* wq = wl + ((u + 1) % D_R) * 576;
      const int c = q & 1, n = c ^ 1;
#pragma unroll
      for (int s = 0; s < 2; ++s) {
#pragma unroll
        for (int nt = 0; nt < 4; ++nt) {
          if (rd) wf[n][4 * s + nt] = wq[(4 * s + nt) * 64];
          acc[nt] = mfma32(wf[c][4 * s + nt], hb[q][s], acc[nt]);
          SB();
        }
        if (rd && s == 0) wf[n][8] = wq[8 * 64];
        acc[4 + s] = mfma32(wf[c][8], hb[q][s], acc[4 + s]);
        SB();
        if (!last && !(VAR & 8)) hb[q][s] = hp[(2 * (u + D_PD) + s) * 64];
        SB();
      }
    }
  };
  for (int u0 = 0; u0 < NU - 8; u0 += 8) body(u0, false);
  body(NU - 8, true);
#pragma unroll
  for (int r = 0; r < 4; ++r) acc[4][r] += __shfl(acc[5][r], lane + 32);
  const int tok = rt * 64 + w * 16 + fi;
  float v[5][4];
  float sum = 0.f;
#pragma unroll
  for (int nt = 0; nt < 5; ++nt) {
    const int col = ct * 72 + nt * 16 + fg * 4;
    const bool valid = nt < 4 || fg < 2;
    const int colc = valid ? col : ct * 72;
    u2 xr = *(const u2*)(xresF + xF_index(tok, colc));
    u2 bb = *(const u2*)(b2 + colc);
#pragma unroll
    for (int r = 0; r < 4; ++r) {
      float bv = bfu((bb[r >> 1] >> ((r & 1) * 16)) & 0xffff);
      float xv = bfu((xr[r >> 1] >> ((r & 1) * 16)) & 0xffff);
      float o = rb(rb(acc[nt][r]) + bv);
      o = rb(xv + o);
      v[nt][r] = o;
      sum += valid ? o : 0.f;
    }
    if (valid) {
      u2 o2; o2[0] = pack2(v[nt][0], v[nt][1]); o2[1] = pack2(v[nt][2], v[nt][3]);
      if (FINAL) *(u2*)(xout + (size_t)tok * D_ + col) = o2; else *(u2*)(xout + xF_index(tok, col)) = o2;
    }
  }
  if (!FINAL) {
    sum += __shfl_xor(sum, 16); sum += __shfl_xor(sum, 32);
    const float mean = sum * (1.0f / 72.0f);
    float m2 = 0.f;
#pragma unroll
    for (int nt = 0; nt < 5; ++nt)
#pragma unroll
      for (int r = 0; r < 4; ++r) { float d = v[nt][r] - mean; m2 += (nt < 4 || fg < 2) ? d * d : 0.f; }
    m2 += __shfl_xor(m2, 16); m2 += __shfl_xor(m2, 32);
    if (fg == 0) { float2 st; st.x = mean; st.y = m2; *(float2*)(stats + (tok * 8 + ct) * 2) = st; }
  }
}

__global__ void kPw1(const bf16* __restrict__ w1, bf16* __restrict__ w1F) {
  int idx = blockIdx.x * 256 + threadIdx.x;
  if (idx >= 32 * 18 * 9 * 64) return;
  int lane = idx & 63, j = (idx >> 6) % 9, ks = (idx / (64 * 9)) % 18, ct = idx / (64 * 9 * 18);
  int i = lane & 15, g = lane >> 4;
  int n = 144 * ct + 16 * j + i;
  unsigned short v[8];
#pragma unroll
  for (int e = 0; e < 8; ++e) v[e] = __builtin_bit_cast(unsigned short, w1[(size_t)(32 * ks + 8 * g + e) * F_ + n]);
  u4 o;
#pragma unroll
  for (int e = 0; e < 4; ++e) o[e] = (unsigned)v[2 * e] | ((unsigned)v[2 * e + 1] << 16);
  *(u4*)(w1F + (size_t)idx * 8) = o;
}
__global__ void kPcs1(const bf16* __restrict__ w1, const bf16* __restrict__ b1, float* __restrict__ cs1) {
  int n = blockIdx.x * 256 + threadIdx.x;
  if (n >= F_) return;
  float s = 0;
  for (int k = 0; k < D_; ++k) s += b2f(w1[(size_t)k * F_ + n]);
  cs1[n] = s; cs1[F_ + n] = b2f(b1[n]);
}
__global__ void n_stats(const bf16* x, float* stats) {
  int idx = blockIdx.x * 256 + threadIdx.x;
  if (idx >= S_ * 8) return;
  int row = idx >> 3, c = idx & 7;
  float s = 0;
  for (int k = 0; k < 72; ++k) s += b2f(x[row * D_ + c * 72 + k]);
  float m = s / 72.f, m2 = 0;
  for (int k = 0; k < 72; ++k) { float d = b2f(x[row * D_ + c * 72 + k]) - m; m2 += d * d; }
  stats[idx * 2] = m; stats[idx * 2 + 1] = m2;
}
DI void ln_stats(const float* __restrict__ stats, int tok, float& mean, float& rstd) {
  const f4* p = (const f4*)(stats + tok * 16);
  f4 a = p[0], b = p[1], c = p[2], d = p[3];
  float m = (a[0] + a[2] + b[0] + b[2] + c[0] + c[2] + d[0] + d[2]) * 0.125f;
  float m2 = a[1] + a[3] + b[1] + b[3] + c[1] + c[3] + d[1] + d[3];
  float q = 0.f, t;
  t = a[0] - m; q += t * t; t = a[2] - m; q += t * t; t = b[0] - m; q += t * t; t = b[2] - m; q += t * t;
  t = c[0] - m; q += t * t; t = c[2] - m; q += t * t; t = d[0] - m; q += t * t; t = d[2] - m; q += t * t;
  float var = (m2 + 72.f * q) * (1.0f / 576.0f);
  mean = m; rstd = 1.0f / sqrtf(var + 1e-5f);
}

#define C_R 16
__global__ __launch_bounds__(704, 3) void kC(const bf16* __restrict__ xF, const bf16* __restrict__ w1F, const float* __restrict__ cs1,
                                             float* __restrict__ stats, bf16* __restrict__ hF) {
  __shared__ __attribute__((aligned(16))) char smem[C_R * 9216];
  const int tid = threadIdx.x, lane = tid & 63, w = tid >> 6;
  const int xcd = blockIdx.x & 7, li = blockIdx.x >> 3;
  const int rt = (xcd & 1) * 4 + (li & 3), ct = (xcd >> 1) * 8 + (li >> 2);
  constexpr int NU = 18;
  if (w >= 8) {
    const int lw = w - 8;
    const char* src = (const char*)w1F + (size_t)ct * NU * 9216 + lw * 3072 + lane * 16;
    char* dst0 = smem + lw * 3072;
#pragma unroll
    for (int u = 0; u < C_R; ++u)
#pragma unroll
      for (int j = 0; j < 3; ++j) glds16(src + u * 9216 + j * 1024, dst0 + u * 9216 + j * 1024);
#pragma unroll
    for (int u = 0; u < 4; ++u) asm volatile("s_waitcnt vmcnt(%0)\n\ts_barrier" :: "i"(3 * (C_R - 1 - u)) : "memory");
#pragma unroll
    for (int u = C_R; u < NU; ++u)
#pragma unroll
      for (int j = 0; j < 3; ++j) glds16(src + u * 9216 + j * 1024, dst0 + (u - C_R) * 9216 + j * 1024);
#pragma unroll
    for (int u = 4; u < NU; ++u) asm volatile("s_waitcnt vmcnt(%0)\n\ts_barrier" :: "i"(3 * (NU - 1 - u)) : "memory");
    return;
  }
  const int fi = lane & 15, fg = lane >> 4;
  const int tt0 = rt * 16 + w * 2;
  const s8* xp = (const s8*)xF + (size_t)tt0 * 18 * 64 + lane;
  const AS3 s8* wl = (const AS3 s8*)smem + lane;
  constexpr int PD = 3;
  s8 xb[PD][2];
#pragma unroll
  for (int p = 0; p < PD; ++p) { xb[p][0] = xp[p * 64]; SB(); xb[p][1] = xp[(18 + p) * 64]; SB(); }
  float mean[2], rstd[2];
#pragma unroll
  for (int mt = 0; mt < 2; ++mt) { ln_stats(stats, (tt0 + mt) * 16 + fi, mean[mt], rstd[mt]); asm volatile("" : "+v"(mean[mt]), "+v"(rstd[mt])); }
  f4 acc[9][2];
#pragma unroll
  for (int a = 0; a < 9; ++a) { acc[a][0] = (f4){0, 0, 0, 0}; acc[a][1] = (f4){0, 0, 0, 0}; }
  s8 wf[9];
  BAR0();
#pragma unroll
  for (int j = 0; j < 9; ++j) wf[j] = wl[j * 64];
  SB();
#pragma unroll
  for (int u = 0; u < NU; ++u) {
    const bool rd = u + 1 < NU;
    if (rd) BAR0();
    const int b = u % PD;
#pragma unroll
    for (int j = 0; j < 9; ++j) {
      acc[j][0] = mfma32(wf[j], xb[b][0], acc[j][0]);
      acc[j][1] = mfma32(wf[j], xb[b][1], acc[j][1]);
      SB();
      if (rd) wf[j] = wl[(((u + 1) % C_R) * 9 + j) * 64];
      SB();
    }
    if (u + PD < NU) { xb[b][0] = xp[(u + PD) * 64]; SB(); xb[b][1] = xp[(18 + u + PD) * 64]; }
    SB();
  }
  unsigned pk[9][2][2];
  float nrm[2] = {-mean[0] * rstd[0], -mean[1] * rstd[1]};
#pragma unroll
  for (int j = 0; j < 9; ++j) {
    const int n = ct * 144 + j * 16 + fg * 4;
    f4 cs = *(const f4*)(cs1 + n), bb = *(const f4*)(cs1 + F_ + n);
#pragma unroll
    for (int mt = 0; mt < 2; ++mt) {
#pragma unroll
      for (int r = 0; r < 2; ++r) {
        f2 a = {acc[j][mt][2 * r], acc[j][mt][2 * r + 1]}, c2 = {cs[2 * r], cs[2 * r + 1]}, b2 = {bb[2 * r], bb[2 * r + 1]};
        f2 t = c2 * nrm[mt] + a * rstd[mt];
        t = unpk(cvtpk(t)) + b2;
        unsigned u = cvtpk(t);
        u = __builtin_bit_cast(unsigned, __builtin_elementwise_max(__builtin_bit_cast(s2, u), (s2){0, 0}));
        t = unpk(u);
        pk[j][mt][r] = cvtpk(t * t);
      }
    }
    SB();
  }
#pragma unroll
  for (int mt = 0; mt < 2; ++mt) {
    char* hb = (char*)hF + ((size_t)(tt0 + mt) * 144 * 64 + lane) * 16;
    const int G0 = 9 * ct;
    if (!(ct & 1)) {
#pragma unroll
      for (int p = 0; p < 4; ++p) { u4 o = {pk[2 * p][mt][0], pk[2 * p][mt][1], pk[2 * p + 1][mt][0], pk[2 * p + 1][mt][1]}; *(u4*)(hb + (size_t)((G0 >> 1) + p) * 1024) = o; }
      u2 o = {pk[8][mt][0], pk[8][mt][1]}; *(u2*)(hb + (size_t)((G0 >> 1) + 4) * 1024) = o;
    } else {
      u2 o = {pk[0][mt][0], pk[0][mt][1]}; *(u2*)(hb + (size_t)(G0 >> 1) * 1024 + 8) = o;
#pragma unroll
      for (int p = 0; p < 4; ++p) { u4 o4 = {pk[2 * p + 1][mt][0], pk[2 * p + 1][mt][1], pk[2 * p + 2][mt][0], pk[2 * p + 2][mt][1]}; *(u4*)(hb + (size_t)((G0 >> 1) + 1 + p) * 1024) = o4; }
    }
  }
}

DI int qkv_col(int hp, int j, int i) {
  if (j < 6) return 576 * (j >> 1) + 18 * (2 * hp + (j & 1)) + i;
  if (i >= 12) return -1;
  int t = i >> 1;
  return 576 * (t >> 1) + 18 * (2 * hp + (t & 1)) + 16 + (i & 1);
}
__global__ void kPqkv(const bf16* __restrict__ w, bf16* __restrict__ wF) {
  int idx = blockIdx.x * 256 + threadIdx.x;
  if (idx >= 16 * 18 * 7 * 64) return;
  int lane = idx & 63, j = (idx >> 6) % 7, ks = (idx / (64 * 7)) % 18, hp = idx / (64 * 7 * 18);
  int i = lane & 15, g = lane >> 4;
  int n = qkv_col(hp, j, i);
  unsigned short v[8];
#pragma unroll
  for (int e = 0; e < 8; ++e) v[e] = n < 0 ? 0 : __builtin_bit_cast(unsigned short, w[(size_t)(32 * ks + 8 * g + e) * (3 * D_) + n]);
  u4 o;
#pragma unroll
  for (int e = 0; e < 4; ++e) o[e] = (unsigned)v[2 * e] | ((unsigned)v[2 * e + 1] << 16);
  *(u4*)(wF + (size_t)idx * 8) = o;
}
__global__ void kPcsA(const bf16* __restrict__ w, float* __restrict__ cs) {
  int idx = blockIdx.x * 256 + threadIdx.x;
  if (idx >= 16 * 112) return;
  int hp = idx / 112, j = (idx % 112) >> 4, i = idx & 15;
  int n = qkv_col(hp, j, i);
  float s = 0;
  if (n >= 0) for (int k = 0; k < D_; ++k) s += b2f(w[(size_t)k * (3 * D_) + n]);
  cs[idx] = s;
}
__global__ void kPphi(const bf16* __restrict__ wphi, bf16* __restrict__ wF) {
  int idx = blockIdx.x * 256 + threadIdx.x;
  if (idx >= 16 * 7 * 512) return;
  int e = idx & 7, lane = (idx >> 3) & 63, f = (idx >> 9) % 7, hp = idx / (7 * 512);
  int i = lane & 15, g = lane >> 4;
  float val = 0.f;
  auto W = [&](int b, int d, int ft) { return b2f(wphi[((2 * hp + (b & 1)) * DH_ + d) * DH_ + ft]); };
  if (f < 4) {
    int b = f;
    if (e < 4) val = W(b, 4 * g + e, i);
    else { int rho = 4 * g + e - 4; if ((rho >> 1) == b) val = W(b, 16 + (rho & 1), i); }
  } else {
    int c = i & 3, sl = i >> 2, b = sl ^ (2 * (hp & 1));
    if (c < 2) {
      if (f == 4) { if (b == (e >> 2)) val = W(b, 4 * g + (e & 3), 16 + c); }
      else if (f == 5) { if (b == 2 + (e >> 2)) val = W(b, 4 * g + (e & 3), 16 + c); }
      else if (e < 4) { int rho = 4 * g + e; if ((rho >> 1) == b) val = W(b, 16 + (rho & 1), 16 + c); }
    }
  }
  wF[idx] = f2b(val);
}
DI int wo_k(int s, int g, int e) {
  if (s < 16) return 18 * (2 * s + (e >> 2)) + 4 * g + (e & 3);
  int q = 4 * (s - 16) + (e >> 1);
  return 18 * (4 * q + g) + 16 + (e & 1);
}
__global__ void kPwo(const bf16* __restrict__ wo, bf16* __restrict__ woF) {
  int idx = blockIdx.x * 256 + threadIdx.x;
  if (idx >= 8 * 9 * 9 * 64) return;
  int lane = idx & 63, u = (idx >> 6) % 9, kp = (idx / (64 * 9)) % 9, ct = idx / (64 * 9 * 9);
  int i = lane & 15, g = lane >> 4;
  int ks, n;
  if (u < 8) { ks = 2 * kp + (u >> 2); n = 72 * ct + 16 * (u & 3) + i; } else { ks = 2 * kp + (i >> 3); n = 72 * ct + 64 + (i & 7); }
  unsigned short v[8];
#pragma unroll
  for (int e = 0; e < 8; ++e) v[e] = __builtin_bit_cast(unsigned short, wo[(size_t)wo_k(ks, g, e) * D_ + n]);
  u4 o;
#pragma unroll
  for (int e = 0; e < 4; ++e) o[e] = (unsigned)v[2 * e] | ((unsigned)v[2 * e + 1] << 16);
  *(u4*)(woF + (size_t)idx * 8) = o;
}

DI float red_max(float x) {
  auto a = __builtin_amdgcn_permlane16_swap(__float_as_uint(x), __float_as_uint(x), false, false);
  x = fmaxf(__uint_as_float(a[0]), __uint_as_float(a[1]));
  auto b = __builtin_amdgcn_permlane32_swap(__float_as_uint(x), __float_as_uint(x), false, false);
  return fmaxf(__uint_as_float(b[0]), __uint_as_float(b[1]));
}
DI float red_min(float x) {
  auto a = __builtin_amdgcn_permlane16_swap(__float_as_uint(x), __float_as_uint(x), false, false);
  x = fminf(__uint_as_float(a[0]), __uint_as_float(a[1]));
  auto b = __builtin_amdgcn_permlane32_swap(__float_as_uint(x), __float_as_uint(x), false, false);
  return fminf(__uint_as_float(b[0]), __uint_as_float(b[1]));
}
DI float red_sum(float x) {
  auto a = __builtin_amdgcn_permlane16_swap(__float_as_uint(x), __float_as_uint(x), false, false);
  x = __uint_as_float(a[0]) + __uint_as_float(a[1]);
  auto b = __builtin_amdgcn_permlane32_swap(__float_as_uint(x), __float_as_uint(x), false, false);
  return __uint_as_float(b[0]) + __uint_as_float(b[1]);
}
DI s8 mk8(unsigned a, unsigned b, unsigned c, unsigned d) { u4 v = {a, b, c, d}; return __builtin_bit_cast(s8, v); }
DI s8 zx8(s4 a) { return __builtin_shufflevector(a, (s4){0, 0, 0, 0}, 0, 1, 2, 3, 4, 5, 6, 7); }
DI s4 trread(const AS3 char* p) { return __builtin_amdgcn_ds_read_tr16_b64_v4i16((AS3 s4*)p); }
#define KV_STRIDE 704

__global__ __launch_bounds__(704, 3) void kA(const bf16* __restrict__ xF, const bf16* __restrict__ wqkvF, const float* __restrict__ csA,
                                             const bf16* __restrict__ wphiF, const float* __restrict__ stats,
                                             bf16* __restrict__ phiQ, bf16* __restrict__ phiQt, float* __restrict__ kvacc) {
  __shared__ __attribute__((aligned(16))) char smem[126 * 1024];
  const int tid = threadIdx.x, lane = tid & 63, w = tid >> 6;
  const int xcd = blockIdx.x & 7, li = blockIdx.x >> 3;
  const int rt = (xcd & 3) * 4 + (li & 3), hp = (xcd >> 2) * 8 + (li >> 2);
  if (w >= 8) {
    const int lw = w - 8;
    const char* src = (const char*)wqkvF + (size_t)hp * 126 * 1024 + lw * 1024 + lane * 16;
    char* dst = smem + lw * 1024;
#pragma unroll
    for (int i = 0; i < 42; ++i) glds16(src + i * 3072, dst + i * 3072);
#pragma unroll
    for (int u = 0; u < 18; ++u) asm volatile("s_waitcnt vmcnt(%0)\n\ts_barrier" :: "i"(42 - (7 * (u + 1) + 2) / 3) : "memory");
    return;
  }
  const int fi = lane & 15, fg = lane >> 4;
  const int tt = rt * 8 + w;
  const s8* xp = (const s8*)xF + (size_t)tt * 18 * 64 + lane;
  const AS3 s8* wl = (const AS3 s8*)smem + lane;
  s8 xb[18];
#pragma unroll
  for (int p = 0; p < 18; ++p) { xb[p] = xp[p * 64]; SB(); }
  float mean, rstd;
  ln_stats(stats, tt * 16 + fi, mean, rstd);
  asm volatile("" : "+v"(mean), "+v"(rstd));
  f4 acc[7];
#pragma unroll
  for (int a = 0; a < 7; ++a) acc[a] = (f4){0, 0, 0, 0};
  s8 wf[7], wp[7];
  BAR0();
#pragma unroll
  for (int j = 0; j < 7; ++j) wf[j] = wl[j * 64];
  SB();
#pragma unroll
  for (int u = 0; u < 18; ++u) {
    const bool rd = u + 1 < 18;
    if (rd) BAR0();
#pragma unroll
    for (int j = 0; j < 7; ++j) {
      acc[j] = mfma32(wf[j], xb[u], acc[j]);
      SB();
      if (rd) wf[j] = wl[((u + 1) * 7 + j) * 64];
      SB();
    }
    if (u == 10) {
#pragma unroll
      for (int f = 0; f < 7; ++f) wp[f] = ((const s8*)wphiF)[(hp * 7 + f) * 64 + lane];
      SB();
    }
  }
  unsigned pk[7][2];
  const float nrm = -mean * rstd;
#pragma unroll
  for (int j = 0; j < 7; ++j) {
    f4 cs = *(const f4*)(csA + hp * 112 + j * 16 + fg * 4);
#pragma unroll
    for (int r = 0; r < 2; ++r) {
      f2 a = {acc[j][2 * r], acc[j][2 * r + 1]}, c2 = {cs[2 * r], cs[2 * r + 1]};
      pk[j][r] = cvtpk(c2 * nrm + a * rstd);
    }
  }
  const f4 z4 = {0, 0, 0, 0};
  f4 P[4], Pt;
#pragma unroll
  for (int b = 0; b < 4; ++b) P[b] = mfma32(wp[b], mk8(pk[b][0], pk[b][1], pk[6][0], pk[6][1]), z4);
  Pt = mfma32(wp[4], mk8(pk[0][0], pk[0][1], pk[1][0], pk[1][1]), z4);
  Pt = mfma32(wp[5], mk8(pk[2][0], pk[2][1], pk[3][0], pk[3][1]), Pt);
  Pt = mfma32(wp[6], mk8(pk[6][0], pk[6][1], pk[6][0], pk[6][1]), Pt);
  const int myb = fg ^ (2 * (hp & 1));
  const f2 tl = unpk(cvtpk((f2){Pt[0], Pt[1]}));
  float p[4][4], mx[4], mn[4];
#pragma unroll
  for (int b = 0; b < 4; ++b) {
    f2 a = unpk(cvtpk((f2){P[b][0], P[b][1]})), c = unpk(cvtpk((f2){P[b][2], P[b][3]}));
    p[b][0] = a[0]; p[b][1] = a[1]; p[b][2] = c[0]; p[b][3] = c[1];
    float hi = fmaxf(fmaxf(a[0], a[1]), fmaxf(c[0], c[1])), lo = fminf(fminf(a[0], a[1]), fminf(c[0], c[1]));
    float hi2 = fmaxf(hi, fmaxf(tl[0], tl[1])), lo2 = fminf(lo, fminf(tl[0], tl[1]));
    hi = myb == b ? hi2 : hi; lo = myb == b ? lo2 : lo;
    mx[b] = red_max(hi); mn[b] = red_min(lo);
  }
  const float L2E = 1.4426950408889634f;
  const float mxo = myb == 0 ? mx[0] : myb == 1 ? mx[1] : myb == 2 ? mx[2] : mx[3];
  const float mno = myb == 0 ? mn[0] : myb == 1 ? mn[1] : myb == 2 ? mn[2] : mn[3];
  float tp[2], tn[2];
#pragma unroll
  for (int c = 0; c < 2; ++c) { tp[c] = __builtin_amdgcn_exp2f((tl[c] - mxo) * L2E); tn[c] = __builtin_amdgcn_exp2f((mno - tl[c]) * L2E); }
  unsigned om[4][4], ot[2];
  float ipo = 0.f, ino = 0.f;
#pragma unroll
  for (int b = 0; b < 4; ++b) {
    float ep[4], en[4];
#pragma unroll
    for (int r = 0; r < 4; ++r) { ep[r] = __builtin_amdgcn_exp2f((p[b][r] - mx[b]) * L2E); en[r] = __builtin_amdgcn_exp2f((mn[b] - p[b][r]) * L2E); }
    float sp = (ep[0] + ep[1]) + (ep[2] + ep[3]), sn = (en[0] + en[1]) + (en[2] + en[3]);
    sp += myb == b ? tp[0] + tp[1] : 0.f; sn += myb == b ? tn[0] + tn[1] : 0.f;
    const float ip = 1.0f / red_sum(sp), in = 1.0f / red_sum(sn);
    ipo = myb == b ? ip : ipo; ino = myb == b ? in : ino;
    om[b][0] = cvtpk((f2){ep[0] * ip, ep[1] * ip}); om[b][1] = cvtpk((f2){ep[2] * ip, ep[3] * ip});
    om[b][2] = cvtpk((f2){en[0] * in, en[1] * in}); om[b][3] = cvtpk((f2){en[2] * in, en[3] * in});
  }
  ot[0] = cvtpk((f2){tp[0] * ipo, tp[1] * ipo}); ot[1] = cvtpk((f2){tn[0] * ino, tn[1] * ino});
#pragma unroll
  for (int b = 0; b < 2; ++b) { u4 o = {om[b][0], om[b][1], om[b][2], om[b][3]}; *(u4*)((char*)phiQ + (((size_t)tt * 32 + 2 * hp + b) * 64 + lane) * 16) = o; }
  if (myb < 2) { u2 o = {ot[0], ot[1]}; *(u2*)((char*)phiQt + (((size_t)tt * 8 + (hp >> 1)) * 64 + lane) * 8) = o; }
  BARRIER();
  {
    AS3 char* row0 = (AS3 char*)smem + (w * 16 + fi) * 160;
#pragma unroll
    for (int hh = 0; hh < 2; ++hh) {
      AS3 char* row = row0 + hh * 128 * 160;
      u4 o = {om[2 + hh][0], om[2 + hh][1], om[2 + hh][2], om[2 + hh][3]};
      *(AS3 u4*)(row + fg * 16) = o;
      if (myb == 2 + hh) { u2 t = {ot[0], ot[1]}; *(AS3 u2*)(row + 64) = t; }
      u2 vv = {pk[4 + hh][0], pk[4 + hh][1]};
      *(AS3 u2*)(row + 80 + fg * 8) = vv;
      if (fg == 2) { u2 t = {pk[6][hh], 0x00003f80u}; *(AS3 u2*)(row + 112) = t; }
    }
  }
  BARRIER();
  if (w < 6) {
    const int hh = w & 1, mt = w >> 1;
    const AS3 char* base = (const AS3 char*)smem + (hh * 128 + 4 * fg + (fi >> 2)) * 160 + (fi & 3) * 8;
    f4 kv[2] = {z4, z4};
#pragma unroll
    for (int ks = 0; ks < 4; ++ks) {
      const AS3 char* bk = base + ks * 32 * 160;
      s4 a0 = trread(bk + mt * 32), a1 = trread(bk + mt * 32 + 16 * 160);
      s8 a = __builtin_shufflevector(a0, a1, 0, 1, 2, 3, 4, 5, 6, 7);
#pragma unroll
      for (int nt = 0; nt < 2; ++nt) {
        s4 b0 = trread(bk + 80 + nt * 32), b1 = trread(bk + 80 + nt * 32 + 16 * 160);
        kv[nt] = mfma32(a, __builtin_shufflevector(b0, b1, 0, 1, 2, 3, 4, 5, 6, 7), kv[nt]);
      }
    }
    float* kp = kvacc + (2 * hp + hh) * KV_STRIDE;
#pragma unroll
    for (int r = 0; r < 4; ++r) {
      const int slot = 16 * mt + 4 * fg + r;
      if (slot < 36) {
        const int a0 = slot < 32 ? ((slot >> 3) * 16 + fi) * 8 + (slot & 7) : 512 + 4 * fi + (slot - 32);
        unsafeAtomicAdd(kp + a0, kv[0][r]);
        if (fi < 3) unsafeAtomicAdd(kp + 576 + fi * 40 + slot, kv[1][r]);
      }
    }
  }
}

__global__ __launch_bounds__(448, 2) void kB(const bf16* __restrict__ phiQ, const bf16* __restrict__ phiQt, const float* __restrict__ kvacc,
                                             const bf16* __restrict__ woF, const bf16* __restrict__ xresF, bf16* __restrict__ xout, float* __restrict__ stats) {
  __shared__ __attribute__((aligned(16))) char smem[81 * 1024 + 32 * KV_STRIDE * 2];
  const int tid = threadIdx.x, lane = tid & 63, w = tid >> 6;
  int bid = blockIdx.x;
  bid = (bid & 7) * 32 + (bid >> 3);
  const int rt = bid >> 3, ct = bid & 7;
  if (w >= 4) {
    const int lw = w - 4;
    const char* src = (const char*)woF + (size_t)ct * 81 * 1024 + lw * 3072 + lane * 16;
    char* dst0 = smem + lw * 3072;
#pragma unroll
    for (int u = 0; u < 9; ++u)
#pragma unroll
      for (int j = 0; j < 3; ++j) glds16(src + u * 9216 + j * 1024, dst0 + u * 9216 + j * 1024);
#pragma unroll
    for (int u = 0; u < 9; ++u) asm volatile("s_waitcnt vmcnt(%0)\n\ts_barrier" :: "i"(3 * (8 - u)) : "memory");
    return;
  }
  const int fi = lane & 15, fg = lane >> 4;
  const int tt = rt * 4 + w;
  AS3 char* kvl = (AS3 char*)smem + 81 * 1024;
  {
    f4 a[11], b[11];
#pragma unroll
    for (int i = 0; i < 11; ++i) { const f4* p = (const f4*)(kvacc + (i * 256 + tid) * 8); a[i] = p[0]; b[i] = p[1]; }
#pragma unroll
    for (int i = 0; i < 11; ++i) {
      u4 o = {cvtpk((f2){a[i][0], a[i][1]}), cvtpk((f2){a[i][2], a[i][3]}), cvtpk((f2){b[i][0], b[i][1]}), cvtpk((f2){b[i][2], b[i][3]})};
      *(AS3 u4*)(kvl + (i * 256 + tid) * 16) = o;
    }
  }
  SB();
  const s8* pp = (const s8*)phiQ + (size_t)tt * 32 * 64 + lane;
  s8 pb[8];
#pragma unroll
  for (int h = 0; h < 8; ++h) { pb[h] = pp[h * 64]; SB(); }
  s4 pt[8];
#pragma unroll
  for (int q = 0; q < 8; ++q) pt[q] = ((const s4*)phiQt)[((size_t)tt * 8 + q) * 64 + lane];
  SB();
  int o1[4], o2[4], o3[4];
#pragma unroll
  for (int m = 0; m < 4; ++m) {
    const int row = fi == 4 * m ? 0 : fi == 4 * m + 1 ? 1 : (fi & 3) == 2 ? 2 : -1;
    o1[m] = fg == m ? 1024 + fi * 8 : 1392;
    o2[m] = row >= 0 ? 1152 + row * 80 + fg * 16 : 1392;
    o3[m] = (row >= 0 && fg == m) ? 1152 + row * 80 + 64 : 1392;
  }
  const AS3 s8* wl = (const AS3 s8*)smem + lane;
  f4 acc[6];
#pragma unroll
  for (int a = 0; a < 6; ++a) acc[a] = (f4){0, 0, 0, 0};
  const f4 z4 = {0, 0, 0, 0};
  unsigned treg[8];
  BARRIER();
#pragma unroll
  for (int u = 0; u < 9; ++u) {
    if (u > 0) BAR0();
    s8 ob[2];
    if (u < 8) {
      unsigned po[4][2];
      unsigned tr = 0;
      const s8 ptz = zx8(pt[u]);
#pragma unroll
      for (int m = 0; m < 4; ++m) {
        const int h = 4 * u + m;
        const AS3 char* kb = kvl + h * (KV_STRIDE * 2);
        s8 f0 = *(const AS3 s8*)(kb + lane * 16);
        s4 f1 = *(const AS3 s4*)(kb + o1[m]);
        s8 f2_ = *(const AS3 s8*)(kb + o2[m]);
        s4 f3 = *(const AS3 s4*)(kb + o3[m]);
        f4 om = mfma32(f0, pb[h & 7], z4);
        om = mfma32(zx8(f1), ptz, om);
        f4 otl = mfma32(f2_, pb[h & 7], z4);
        otl = mfma32(zx8(f3), ptz, otl);
        if (h + 8 < 32) pb[h & 7] = pp[(h + 8) * 64];
        const float den = rb(rb(otl[2]) + 1e-6f);
        const float inv = 1.0f / den;
        f2 a = unpk(cvtpk((f2){om[0], om[1]})), c = unpk(cvtpk((f2){om[2], om[3]})), t = unpk(cvtpk((f2){otl[0], otl[1]}));
        po[m][0] = cvtpk(a * inv); po[m][1] = cvtpk(c * inv);
        unsigned tv = cvtpk(t * inv);
        tr = fg == m ? tv : tr;
      }
      treg[u] = tr;
      ob[0] = mk8(po[0][0], po[0][1], po[1][0], po[1][1]);
      ob[1] = mk8(po[2][0], po[2][1], po[3][0], po[3][1]);
    } else {
      ob[0] = mk8(treg[0], treg[1], treg[2], treg[3]);
      ob[1] = mk8(treg[4], treg[5], treg[6], treg[7]);
    }
    s8 wt = wl[(u * 9 + 8) * 64];
#pragma unroll
    for (int s = 0; s < 2; ++s) {
#pragma unroll
      for (int nt = 0; nt < 4; ++nt) acc[nt] = mfma32(wl[(u * 9 + 4 * s + nt) * 64], ob[s], acc[nt]);
      acc[4 + s] = mfma32(wt, ob[s], acc[4 + s]);
    }
  }
#pragma unroll
  for (int r = 0; r < 4; ++r) acc[4][r] += __shfl(acc[5][r], lane + 32);
  const int tok = tt * 16 + fi;
  float v[5][4];
  float sum = 0.f;
#pragma unroll
  for (int nt = 0; nt < 5; ++nt) {
    const int col = ct * 72 + nt * 16 + fg * 4;
    const bool valid = nt < 4 || fg < 2;
    const int colc = valid ? col : ct * 72;
    u2 xr = *(const u2*)(xresF + xF_index(tok, colc));
#pragma unroll
    for (int r = 0; r < 4; ++r) {
      float xv = bfu((xr[r >> 1] >> ((r & 1) * 16)) & 0xffff);
      float o = rb(xv + rb(acc[nt][r]));
      v[nt][r] = o;
      sum += valid ? o : 0.f;
    }
    if (valid) {
      u2 o2_; o2_[0] = pack2(v[nt][0], v[nt][1]); o2_[1] = pack2(v[nt][2], v[nt][3]);
      *(u2*)(xout + xF_index(tok, col)) = o2_;
    }
  }
  sum = red_sum(sum);
  const float mean = sum * (1.0f / 72.0f);
  float m2 = 0.f;
#pragma unroll
  for (int nt = 0; nt < 5; ++nt)
#pragma unroll
    for (int r = 0; r < 4; ++r) { float d = v[nt][r] - mean; m2 += (nt < 4 || fg < 2) ? d * d : 0.f; }
  m2 = red_sum(m2);
  if (fg == 0) { float2 st; st.x = mean; st.y = m2; *(float2*)(stats + (tok * 8 + ct) * 2) = st; }
}

extern "C" void kernel_launch(void* const* d_in, const int* in_sizes, int n_in,
                              void* d_out, int out_size, void* d_ws, size_t ws_size,
                              hipStream_t stream) {
  const bf16* x0 = (const bf16*)d_in[0];
  const bf16* wqkv = (const bf16*)d_in[1];
  const bf16* wphi = (const bf16*)d_in[2];
  const bf16* wo = (const bf16*)d_in[3];
  const bf16* w1 = (const bf16*)d_in[5];
  const bf16* b1 = (const bf16*)d_in[6];
  const bf16* w2 = (const bf16*)d_in[7];
  const bf16* b2 = (const bf16*)d_in[8];
  char* ws = (char*)d_ws;
  size_t off = 0;
  auto alloc = [&](size_t bytes) { void* p = ws + off; off += (bytes + 255) & ~(size_t)255; return p; };
  bf16* x0F = (bf16*)alloc(S_ * D_ * 2);
  bf16* x1F = (bf16*)alloc(S_ * D_ * 2);
  bf16* x2F = (bf16*)alloc(S_ * D_ * 2);
  bf16* h = (bf16*)alloc((size_t)S_ * F_ * 2);
  bf16* w2F = (bf16*)alloc((size_t)8 * 72 * 9 * 1024);
  bf16* w1F = (bf16*)alloc((size_t)D_ * F_ * 2);
  bf16* wqkvF = (bf16*)alloc((size_t)16 * 126 * 1024);
  bf16* woF = (bf16*)alloc((size_t)8 * 81 * 1024);
  bf16* wphiF = (bf16*)alloc((size_t)16 * 7 * 1024);
  bf16* phiQ = (bf16*)alloc((size_t)128 * 32 * 1024);
  bf16* phiQt = (bf16*)alloc((size_t)128 * 8 * 512);
  float* kvacc = (float*)alloc(2 * 32 * KV_STRIDE * 4);
  float* cs1 = (float*)alloc(F_ * 8);
  float* csA = (float*)alloc(16 * 112 * 4);
  float* stats1 = (float*)alloc(S_ * 16 * 4 + 65536);
  float* stats = (float*)alloc(S_ * 16 * 4 + 65536);
  kPw2<<<(8 * 72 * 9 * 64 + 255) / 256, 256, 0, stream>>>(w2, w2F);
  kPw1<<<(32 * 18 * 9 * 64 + 255) / 256, 256, 0, stream>>>(w1, w1F);
  kPcs1<<<F_ / 256, 256, 0, stream>>>(w1, b1, cs1);
  kPqkv<<<(16 * 18 * 7 * 64 + 255) / 256, 256, 0, stream>>>(wqkv, wqkvF);
  kPcsA<<<7, 256, 0, stream>>>(wqkv, csA);
  kPphi<<<(16 * 7 * 512 + 255) / 256, 256, 0, stream>>>(wphi, wphiF);
  kPwo<<<(8 * 9 * 9 * 64 + 255) / 256, 256, 0, stream>>>(wo, woF);
  (void)hipMemsetAsync(kvacc, 0, 2 * 32 * KV_STRIDE * 4, stream);
  n_toF<<<S_ * D_ / 256, 256, 0, stream>>>(x0, x0F);
  n_stats<<<S_ * 8 / 256, 256, 0, stream>>>(x0, stats);
  const bf16* xin = x0F;
  for (int layer = 0; layer < 2; ++layer) {
    float* kv = kvacc + layer * 32 * KV_STRIDE;
    kA<<<256, 704, 0, stream>>>(xin, wqkvF, csA, wphiF, stats, phiQ, phiQt, kv);
    kB<<<256, 448, 0, stream>>>(phiQ, phiQt, kv, woF, xin, x1F, stats1);
    kC<<<256, 704, 0, stream>>>(x1F, w1F, cs1, stats1, h);
    if (layer == 0) kD<false><<<256, 448, 0, stream>>>(h, w2F, b2, x1F, x2F, stats);
    else kD<true><<<256, 448, 0, stream>>>(h, w2F, b2, x1F, (bf16*)d_out, stats);
    xin = x2F;
  }
}
```

```cpp
#include <hip/hip_runtime.h>
#include <hip/hip_bf16.h>
#include <stdint.h>

typedef __hip_bfloat16 bf16;
typedef short s4 __attribute__((ext_vector_type(4)));
typedef short s8 __attribute__((ext_vector_type(8)));
typedef float f4 __attribute__((ext_vector_type(4)));
typedef __bf16 bf8 __attribute__((ext_vector_type(8)));
typedef unsigned u4 __attribute__((ext_vector_type(4)));
typedef unsigned u2 __attribute__((ext_vector_type(2)));
#define AS3 __attribute__((address_space(3)))
#define AS1 __attribute__((address_space(1)))
#define DI __device__ __forceinline__

#define S_ 2048
#define D_ 576
#define H_ 32
#define DH_ 18
#define F_ 4608

DI float b2f(bf16 x) { return __bfloat162float(x); }
DI bf16 f2b(float x) { return __float2bfloat16(x); }
DI float rb(float x) { return b2f(f2b(x)); }
DI float bfu(unsigned short u) { return __uint_as_float(((unsigned)u) << 16); }
DI unsigned pack2(float a, float b) {
  bf16 x = f2b(a), y = f2b(b);
  return (unsigned)__builtin_bit_cast(unsigned short, x) | ((unsigned)__builtin_bit_cast(unsigned short, y) << 16);
}
typedef float f2 __attribute__((ext_vector_type(2)));
typedef short s2 __attribute__((ext_vector_type(2)));
typedef __bf16 bf2 __attribute__((ext_vector_type(2)));
DI unsigned cvtpk(f2 v) { return __builtin_bit_cast(unsigned, __builtin_convertvector(v, bf2)); }
DI f2 unpk(unsigned u) { f2 r; r[0] = __uint_as_float(u << 16); r[1] = __uint_as_float(u & 0xffff0000u); return r; }
DI f4 mfma32(s8 a, s8 b, f4 c) { return __builtin_amdgcn_mfma_f32_16x16x32_bf16(__builtin_bit_cast(bf8, a), __builtin_bit_cast(bf8, b), c, 0, 0, 0); }
DI void mfma_acc(f4& c, s8 a, s8 b) { asm volatile("v_mfma_f32_16x16x32_bf16 %0, %1, %2, %0" : "+v"(c) : "v"(a), "v"(b)); }
#define MFMA_FENCE() asm volatile("s_nop 7\n\ts_nop 3" ::: "memory")
#define LDS_R128(d, addr, off) asm volatile("ds_read_b128 %0, %1 offset:%2" : "=v"(d) : "v"(addr), "i"(off))
#define LDS_RTR(d, addr, off) asm volatile("ds_read_b64_tr_b16 %0, %1 offset:%2" : "=v"(d) : "v"(addr), "i"(off))
DI unsigned lds_addr(const void* p) { return (unsigned)(uintptr_t)(AS3 const char*)p; }
DI const AS3 s8* opaque_lds(const AS3 s8* p) { unsigned a = (unsigned)(uintptr_t)p; asm volatile("" : "+v"(a)); return (const AS3 s8*)(uintptr_t)a; }
DI void glds16(const void* g, char* l) { __builtin_amdgcn_global_load_lds((const AS1 unsigned*)g, (AS3 unsigned*)l, 16, 0, 0); }
#define WAIT_V(n) asm volatile("s_waitcnt vmcnt(" #n ")" ::: "memory")

#define ST_DECL
#define ST(K, i)
#define CY0(K)
#define CY1(K)
#define ST_OUT(K)
DI size_t hF_index(int t, int f) {
  int ks = f >> 5, r = f & 31, hi = r >> 4, g = (r & 15) >> 2, e = (r & 3) | (hi << 2);
  return ((((size_t)(t >> 4) * (F_ / 32) + ks) * 64 + g * 16 + (t & 15)) << 3) + e;
}
DI size_t xF_index(int t, int k) { return ((((size_t)(t >> 4) * 18 + (k >> 5)) * 64 + ((k >> 3) & 3) * 16 + (t & 15)) << 3) + (k & 7); }
#define SB() __builtin_amdgcn_sched_barrier(0)
#define BAR0() asm volatile("s_barrier" ::: "memory")
#define BARRIER() asm volatile("s_waitcnt lgkmcnt(0)\n\ts_barrier" ::: "memory")

#define VAR 0
#define D_R 16
#define D_PD 8
template <bool FINAL>
__global__ __launch_bounds__(448, 2) void kD(const bf16* __restrict__ hF, const bf16* __restrict__ w2F, const bf16* __restrict__ b2,
                                             const bf16* __restrict__ xresF, bf16* __restrict__ xout, float* __restrict__ stats) {
  __shared__ __attribute__((aligned(16))) char smem[D_R * 9216];
  const int tid = threadIdx.x, lane = tid & 63, w = tid >> 6;
  int bid = blockIdx.x;
  bid = (bid & 7) * 32 + (bid >> 3);
  const int rt = bid >> 3, ct = bid & 7;
  constexpr int NU = 72;
  if (w >= 4) {
    if (VAR & 64) { BAR0(); return; }
    const int lw = w - 4;
    const char* src = (const char*)w2F + (size_t)((VAR & 2) ? 0 : ct) * NU * 9216 + lw * 3072 + lane * 16;
    char* dst0 = smem + lw * 3072;
#pragma unroll
    for (int u = 0; u < D_R - 2; ++u)
#pragma unroll
      for (int j = 0; j < 3; ++j) glds16(src + u * 9216 + j * 1024, dst0 + u * 9216 + j * 1024);
    for (int u = 0; u < NU - (D_R - 2); ++u) {
      asm volatile("s_waitcnt vmcnt(%0)\n\ts_barrier" :: "i"(3 * (D_R - 3)) : "memory");
      const int un = u + D_R - 2;
      if (!(VAR & 16)) {
        char* dst = dst0 + (un % D_R) * 9216;
#pragma unroll
        for (int j = 0; j < 3; ++j) glds16(src + un * 9216 + j * 1024, dst + j * 1024);
      }
    }
#pragma unroll
    for (int r = D_R - 3; r >= 0; --r) asm volatile("s_waitcnt vmcnt(%0)\n\ts_barrier" :: "i"(3 * r) : "memory");
    return;
  }
  const int fi = lane & 15, fg = lane >> 4;
  const s8* hp = (const s8*)hF + (size_t)(((VAR & 1) ? 0 : rt) * 4 + w) * 144 * 64 + lane;
  const AS3 s8* wl = (const AS3 s8*)smem + lane;
  f4 acc[6];
#pragma unroll
  for (int a = 0; a < 6; ++a) acc[a] = (f4){0, 0, 0, 0};
  s8 hb[D_PD][2];
#pragma unroll
  for (int p = 0; p < D_PD; ++p) { hb[p][0] = hp[(2 * p) * 64]; SB(); hb[p][1] = hp[(2 * p + 1) * 64]; SB(); }
  s8 wf[2][9];
  BAR0();
#pragma unroll
  for (int j = 0; j < 9; ++j) wf[0][j] = wl[j * 64];
  SB();
  auto body = [&](int u0, bool last) {
#pragma unroll
    for (int q = 0; q < 8; ++q) {
      const int u = u0 + q;
      const bool rd = !last || q < 7;
      if (rd && !(VAR & 64)) BAR0();
      const AS3 s8* wq = wl + ((u + 1) % D_R) * 576;
      const int c = q & 1, n = c ^ 1;
#pragma unroll
      for (int s = 0; s < 2; ++s) {
#pragma unroll
        for (int nt = 0; nt < 4; ++nt) {
          if (rd) wf[n][4 * s + nt] = wq[(4 * s + nt) * 64];
          acc[nt] = mfma32(wf[c][4 * s + nt], hb[q][s], acc[nt]);
          SB();
        }
        if (rd && s == 0) wf[n][8] = wq[8 * 64];
        acc[4 + s] = mfma32(wf[c][8], hb[q][s], acc[4 + s]);
        SB();
        if (!last && !(VAR & 8)) hb[q][s] = hp[(2 * (u + D_PD) + s) * 64];
        SB();
      }
    }
  };
  for (int u0 = 0; u0 < NU - 8; u0 += 8) body(u0, false);
  body(NU - 8, true);
#pragma unroll
  for (int r = 0; r < 4; ++r) acc[4][r] += __shfl(acc[5][r], lane + 32);
  const int tok = rt * 64 + w * 16 + fi;
  float v[5][4];
  float sum = 0.f;
#pragma unroll
  for (int nt = 0; nt < 5; ++nt) {
    const int col = ct * 72 + nt * 16 + fg * 4;
    const bool valid = nt < 4 || fg < 2;
    const int colc = valid ? col : ct * 72;
    u2 xr = *(const u2*)(xresF + xF_index(tok, colc));
    u2 bb = *(const u2*)(b2 + colc);
#pragma unroll
    for (int r = 0; r < 4; ++r) {
      float bv = bfu((bb[r >> 1] >> ((r & 1) * 16)) & 0xffff);
      float xv = bfu((xr[r >> 1] >> ((r & 1) * 16)) & 0xffff);
      float o = rb(rb(acc[nt][r]) + bv);
      o = rb(xv + o);
      v[nt][r] = o;
      sum += valid ? o : 0.f;
    }
    if (valid) {
      u2 o2; o2[0] = pack2(v[nt][0], v[nt][1]); o2[1] = pack2(v[nt][2], v[nt][3]);
      if (FINAL) *(u2*)(xout + (size_t)tok * D_ + col) = o2; else *(u2*)(xout + xF_index(tok, col)) = o2;
    }
  }
  if (!FINAL) {
    sum += __shfl_xor(sum, 16); sum += __shfl_xor(sum, 32);
    const float mean = sum * (1.0f / 72.0f);
    float m2 = 0.f;
#pragma unroll
    for (int nt = 0; nt < 5; ++nt)
#pragma unroll
      for (int r = 0; r < 4; ++r) { float d = v[nt][r] - mean; m2 += (nt < 4 || fg < 2) ? d * d : 0.f; }
    m2 += __shfl_xor(m2, 16); m2 += __shfl_xor(m2, 32);
    if (fg == 0) { float2 st; st.x = mean; st.y = m2; *(float2*)(stats + (tok * 8 + ct) * 2) = st; }
  }
}

DI void ln_comb(const f4 (&p)[4], float& mean, float& rstd);
DI void ln_stats(const float* __restrict__ stats, int tok, float& mean, float& rstd) {
  const f4* p = (const f4*)(stats + tok * 16);
  f4 q[4] = {p[0], p[1], p[2], p[3]};
  ln_comb(q, mean, rstd);
}
DI void ln_comb(const f4 (&p)[4], float& mean, float& rstd) {
  f4 a = p[0], b = p[1], c = p[2], d = p[3];
  float m = (a[0] + a[2] + b[0] + b[2] + c[0] + c[2] + d[0] + d[2]) * 0.125f;
  float m2 = a[1] + a[3] + b[1] + b[3] + c[1] + c[3] + d[1] + d[3];
  float q = 0.f, t;
  t = a[0] - m; q += t * t; t = a[2] - m; q += t * t; t = b[0] - m; q += t * t; t = b[2] - m; q += t * t;
  t = c[0] - m; q += t * t; t = c[2] - m; q += t * t; t = d[0] - m; q += t * t; t = d[2] - m; q += t * t;
  float var = (m2 + 72.f * q) * (1.0f / 576.0f);
  mean = m; rstd = 1.0f / sqrtf(var + 1e-5f);
}

#define C_R 16
__global__ __launch_bounds__(704, 3) void kC(const bf16* __restrict__ xF, const bf16* __restrict__ w1F, const float* __restrict__ cs1,
                                             float* __restrict__ stats, bf16* __restrict__ hF) {
  __shared__ __attribute__((aligned(16))) char smem[C_R * 9216];
  const int tid = threadIdx.x, lane = tid & 63, w = tid >> 6;
  const int xcd = blockIdx.x & 7, li = blockIdx.x >> 3;
  const int rt = (xcd & 1) * 4 + (li & 3), ct = (xcd >> 1) * 8 + (li >> 2);
  constexpr int NU = 18;
  if (w >= 8) {
    const int lw = w - 8;
    const char* src = (const char*)w1F + (size_t)ct * NU * 9216 + lw * 3072 + lane * 16;
    char* dst0 = smem + lw * 3072;
#pragma unroll
    for (int u = 0; u < C_R; ++u)
#pragma unroll
      for (int j = 0; j < 3; ++j) glds16(src + u * 9216 + j * 1024, dst0 + u * 9216 + j * 1024);
#pragma unroll
    for (int u = 0; u < 4; ++u) asm volatile("s_waitcnt vmcnt(%0)\n\ts_barrier" :: "i"(3 * (C_R - 1 - u)) : "memory");
#pragma unroll
    for (int u = C_R; u < NU; ++u)
#pragma unroll
      for (int j = 0; j < 3; ++j) glds16(src + u * 9216 + j * 1024, dst0 + (u - C_R) * 9216 + j * 1024);
#pragma unroll
    for (int u = 4; u < NU; ++u) asm volatile("s_waitcnt vmcnt(%0)\n\ts_barrier" :: "i"(3 * (NU - 1 - u)) : "memory");
    return;
  }
  const int fi = lane & 15, fg = lane >> 4;
  const int tt0 = rt * 16 + w * 2;
  const s8* xp = (const s8*)xF + (size_t)tt0 * 18 * 64 + lane;
  const AS3 s8* wl = (const AS3 s8*)smem + lane;
  constexpr int PD = 3;
  s8 xb[PD][2];
#pragma unroll
  for (int p = 0; p < PD; ++p) { xb[p][0] = xp[p * 64]; SB(); xb[p][1] = xp[(18 + p) * 64]; SB(); }
  float mean[2], rstd[2];
#pragma unroll
  for (int mt = 0; mt < 2; ++mt) { ln_stats(stats, (tt0 + mt) * 16 + fi, mean[mt], rstd[mt]); asm volatile("" : "+v"(mean[mt]), "+v"(rstd[mt])); }
  f4 acc[9][2];
#pragma unroll
  for (int a = 0; a < 9; ++a) { acc[a][0] = (f4){0, 0, 0, 0}; acc[a][1] = (f4){0, 0, 0, 0}; }
  s8 wf[9];
  BAR0();
#pragma unroll
  for (int j = 0; j < 9; ++j) wf[j] = wl[j * 64];
  SB();
#pragma unroll
  for (int u = 0; u < NU; ++u) {
    const bool rd = u + 1 < NU;
    if (rd) BAR0();
    const int b = u % PD;
#pragma unroll
    for (int j = 0; j < 9; ++j) {
      acc[j][0] = mfma32(wf[j], xb[b][0], acc[j][0]);
      acc[j][1] = mfma32(wf[j], xb[b][1], acc[j][1]);
      SB();
      if (rd) wf[j] = wl[(((u + 1) % C_R) * 9 + j) * 64];
      SB();
    }
    if (u + PD < NU) { xb[b][0] = xp[(u + PD) * 64]; SB(); xb[b][1] = xp[(18 + u + PD) * 64]; }
    SB();
  }
  unsigned pk[9][2][2];
  float nrm[2] = {-mean[0] * rstd[0], -mean[1] * rstd[1]};
#pragma unroll
  for (int j = 0; j < 9; ++j) {
    const int n = ct * 144 + j * 16 + fg * 4;
    f4 cs = *(const f4*)(cs1 + n) + *(const f4*)(cs1 + F_ + n) + *(const f4*)(cs1 + 2 * F_ + n), bb = *(const f4*)(cs1 + 3 * F_ + n);
#pragma unroll
    for (int mt = 0; mt < 2; ++mt) {
#pragma unroll
      for (int r = 0; r < 2; ++r) {
        f2 a = {acc[j][mt][2 * r], acc[j][mt][2 * r + 1]}, c2 = {cs[2 * r], cs[2 * r + 1]}, b2 = {bb[2 * r], bb[2 * r + 1]};
        f2 t = c2 * nrm[mt] + a * rstd[mt];
        t = unpk(cvtpk(t)) + b2;
        unsigned u = cvtpk(t);
        u = __builtin_bit_cast(unsigned, __builtin_elementwise_max(__builtin_bit_cast(s2, u), (s2){0, 0}));
        t = unpk(u);
        pk[j][mt][r] = cvtpk(t * t);
      }
    }
    SB();
  }
#pragma unroll
  for (int mt = 0; mt < 2; ++mt) {
    char* hb = (char*)hF + ((size_t)(tt0 + mt) * 144 * 64 + lane) * 16;
    const int G0 = 9 * ct;
    if (!(ct & 1)) {
#pragma unroll
      for (int p = 0; p < 4; ++p) { u4 o = {pk[2 * p][mt][0], pk[2 * p][mt][1], pk[2 * p + 1][mt][0], pk[2 * p + 1][mt][1]}; *(u4*)(hb + (size_t)((G0 >> 1) + p) * 1024) = o; }
      u2 o = {pk[8][mt][0], pk[8][mt][1]}; *(u2*)(hb + (size_t)((G0 >> 1) + 4) * 1024) = o;
    } else {
      u2 o = {pk[0][mt][0], pk[0][mt][1]}; *(u2*)(hb + (size_t)(G0 >> 1) * 1024 + 8) = o;
#pragma unroll
      for (int p = 0; p < 4; ++p) { u4 o4 = {pk[2 * p + 1][mt][0], pk[2 * p + 1][mt][1], pk[2 * p + 2][mt][0], pk[2 * p + 2][mt][1]}; *(u4*)(hb + (size_t)((G0 >> 1) + 1 + p) * 1024) = o4; }
    }
  }
}

DI int qkv_col(int hp, int j, int i) {
  if (j < 6) return 576 * (j >> 1) + 18 * (2 * hp + (j & 1)) + i;
  if (i >= 12) return -1;
  int t = i >> 1;
  return 576 * (t >> 1) + 18 * (2 * hp + (t & 1)) + 16 + (i & 1);
}
DI int wo_k(int s, int g, int e) {
  if (s < 16) return 18 * (2 * s + (e >> 2)) + 4 * g + (e & 3);
  int q = 4 * (s - 16) + (e >> 1);
  return 18 * (4 * q + g) + 16 + (e & 1);
}
DI float red_max(float x) {
  auto a = __builtin_amdgcn_permlane16_swap(__float_as_uint(x), __float_as_uint(x), false, false);
  x = fmaxf(__uint_as_float(a[0]), __uint_as_float(a[1]));
  auto b = __builtin_amdgcn_permlane32_swap(__float_as_uint(x), __float_as_uint(x), false, false);
  return fmaxf(__uint_as_float(b[0]), __uint_as_float(b[1]));
}
DI float red_min(float x) {
  auto a = __builtin_amdgcn_permlane16_swap(__float_as_uint(x), __float_as_uint(x), false, false);
  x = fminf(__uint_as_float(a[0]), __uint_as_float(a[1]));
  auto b = __builtin_amdgcn_permlane32_swap(__float_as_uint(x), __float_as_uint(x), false, false);
  return fminf(__uint_as_float(b[0]), __uint_as_float(b[1]));
}
DI float red_sum(float x) {
  auto a = __builtin_amdgcn_permlane16_swap(__float_as_uint(x), __float_as_uint(x), false, false);
  x = __uint_as_float(a[0]) + __uint_as_float(a[1]);
  auto b = __builtin_amdgcn_permlane32_swap(__float_as_uint(x), __float_as_uint(x), false, false);
  return __uint_as_float(b[0]) + __uint_as_float(b[1]);
}
DI s8 mk8(unsigned a, unsigned b, unsigned c, unsigned d) { u4 v = {a, b, c, d}; return __builtin_bit_cast(s8, v); }
DI s8 zx8(s4 a) { return __builtin_shufflevector(a, (s4){0, 0, 0, 0}, 0, 1, 2, 3, 4, 5, 6, 7); }
DI s4 trread(const AS3 char* p) { return __builtin_amdgcn_ds_read_tr16_b64_v4i16((AS3 s4*)p); }
#define KV_STRIDE 768

DI float max2f(float a, float b) { return __builtin_elementwise_maximum(a, b); }
DI float min2f(float a, float b) { return __builtin_elementwise_minimum(a, b); }
DI float max3f(float a, float b, float c) { return max2f(max2f(a, b), c); }
DI float min3f(float a, float b, float c) { return min2f(min2f(a, b), c); }
DI void swap16(float& a, float& b) { auto r = __builtin_amdgcn_permlane16_swap(__float_as_uint(a), __float_as_uint(b), false, false); a = __uint_as_float(r[0]); b = __uint_as_float(r[1]); }
DI void swap32(float& a, float& b) { auto r = __builtin_amdgcn_permlane32_swap(__float_as_uint(a), __float_as_uint(b), false, false); a = __uint_as_float(r[0]); b = __uint_as_float(r[1]); }
template <int OP> DI float op2(float a, float b) { return OP == 0 ? max2f(a, b) : OP == 1 ? min2f(a, b) : a + b; }
template <int OP> DI float rscat(float a, float b, float c, float d) {
  swap16(a, b); swap16(c, d);
  float x = op2<OP>(a, b), y = op2<OP>(c, d);
  swap32(x, y);
  return op2<OP>(x, y);
}
DI void agath(float r, float (&o)[4]) {
  float x = r, y = r;
  swap16(x, y);
  float x2 = x, y2 = y;
  swap32(x, x2); swap32(y, y2);
  o[0] = x; o[2] = x2; o[1] = y; o[3] = y2;
}
#define A_XW 6
#define A_GD 3
__global__ __launch_bounds__(512, 2) void kA(const bf16* __restrict__ xF, const bf16* __restrict__ wqkvF, const float* __restrict__ csA,
                                             const bf16* __restrict__ wphiF, const float* __restrict__ stats,
                                             bf16* __restrict__ phiQ, bf16* __restrict__ phiQt, float* __restrict__ kvacc) {
  __shared__ __attribute__((aligned(16))) char smem[128 * 1024];
  const int tid = threadIdx.x, lane = tid & 63, w = tid >> 6;
  const int xcd = blockIdx.x & 7, li = blockIdx.x >> 3;
  const int rt = (xcd & 3) * 4 + (li & 3), hp = (xcd >> 2) * 8 + (li >> 2);
  ST_DECL ST(1, 0);
  if (w >= 4) {
    const int lw = w - 4;
    const char* src = (const char*)wqkvF + (size_t)hp * 126 * 1024 + lw * 1024 + lane * 16;
    char* dst = smem + lw * 1024;
    auto cum = [](int g) { return g >= 8 ? 32 : (14 * (g + 1) + 3) / 4; };
#pragma unroll
    for (int i = 0; i < cum(A_GD - 1); ++i) glds16(src + i * 4096, dst + i * 4096);
#pragma unroll
    for (int g = 0; g < 9; ++g) {
      asm volatile("s_waitcnt vmcnt(%0)\n\ts_barrier" :: "i"(cum(g + A_GD - 1) - cum(g)) : "memory");
#pragma unroll
      for (int i = cum(g + A_GD - 1); i < cum(g + A_GD); ++i) glds16(src + i * 4096, dst + i * 4096);
    }
    return;
  }
  const int fi = lane & 15, fg = lane >> 4;
  const int tt0 = rt * 8 + w * 2;
  const s8* xp = (const s8*)xF + (size_t)tt0 * 18 * 64 + lane;
  const AS3 s8* wl = (const AS3 s8*)smem + lane;
  const AS3 s8* wl2 = opaque_lds(wl + 4096);
  auto wrd = [&](int piece) { return piece < 64 ? wl[piece * 64] : wl2[(piece - 64) * 64]; };
  float mean[2], rstd[2];
  f4 acc[7][2];
  s8 xb[18][2];
  {
    f4 st[2][4], cs[7];
#pragma unroll
    for (int mt = 0; mt < 2; ++mt)
#pragma unroll
      for (int q = 0; q < 4; ++q) st[mt][q] = ((const f4*)(stats + ((tt0 + mt) * 16 + fi) * 16))[q];
#pragma unroll
    for (int j = 0; j < 7; ++j) cs[j] = *(const f4*)(csA + hp * 112 + j * 16 + fg * 4);
    SB();
#pragma unroll
    for (int p = 0; p < A_XW; ++p) { xb[p][0] = xp[p * 64]; xb[p][1] = xp[(18 + p) * 64]; SB(); }
#pragma unroll
    for (int mt = 0; mt < 2; ++mt) ln_comb(st[mt], mean[mt], rstd[mt]);
#pragma unroll
    for (int j = 0; j < 7; ++j) { acc[j][0] = cs[j] * (-mean[0]); acc[j][1] = cs[j] * (-mean[1]); }
    SB();
  }
  s8 wf[7], wp[7];
  BAR0();
  ST(1, 1); CY0(1);
#pragma unroll
  for (int j = 0; j < 7; ++j) wf[j] = wl[j * 64];
  SB();
#pragma unroll
  for (int u = 0; u < 18; ++u) {
    const bool rd = u + 1 < 18;
    if (rd && (u & 1)) BAR0();
#pragma unroll
    for (int j = 0; j < 7; ++j) {
      mfma_acc(acc[j][0], wf[j], xb[u][0]);
      SB();
      if (j > 0) { if (rd) wf[j - 1] = wrd((u + 1) * 7 + j - 1); }
      else if (u > 0) wf[6] = wrd(u * 7 + 6);
      SB();
      mfma_acc(acc[j][1], wf[j], xb[u][1]);
      SB();
    }
    if (u + A_XW < 18) { xb[u + A_XW][0] = xp[(u + A_XW) * 64]; xb[u + A_XW][1] = xp[(18 + u + A_XW) * 64]; SB(); }
    if (u == 10) {
#pragma unroll
      for (int f = 0; f < 7; ++f) wp[f] = ((const s8*)wphiF)[(hp * 7 + f) * 64 + lane];
      SB();
    }
  }
  MFMA_FENCE();
  SB();
  ST(1, 2); CY1(1);
  const f4 z4 = {0, 0, 0, 0};
  const float L2E = 1.4426950408889634f;
  AS3 char* stg = (AS3 char*)smem + w * 10240;
  const bool odd = hp & 1;
#pragma unroll
  for (int mt = 0; mt < 2; ++mt) {
    unsigned pk[7][2];
#pragma unroll
    for (int j = 0; j < 7; ++j)
#pragma unroll
      for (int r = 0; r < 2; ++r) pk[j][r] = cvtpk((f2){acc[j][mt][2 * r], acc[j][mt][2 * r + 1]} * rstd[mt]);
    f4 P[4], Pt;
#pragma unroll
    for (int b = 0; b < 4; ++b) P[b] = mfma32(wp[b], mk8(pk[b][0], pk[b][1], pk[6][0], pk[6][1]), z4);
    Pt = mfma32(wp[4], mk8(pk[0][0], pk[0][1], pk[1][0], pk[1][1]), z4);
    Pt = mfma32(wp[5], mk8(pk[2][0], pk[2][1], pk[3][0], pk[3][1]), Pt);
    Pt = mfma32(wp[6], mk8(pk[6][0], pk[6][1], pk[6][0], pk[6][1]), Pt);
    float hi[4], lo[4];
#pragma unroll
    for (int b = 0; b < 4; ++b) { hi[b] = max2f(max3f(P[b][0], P[b][1], P[b][2]), P[b][3]); lo[b] = min2f(min3f(P[b][0], P[b][1], P[b][2]), P[b][3]); }
    const float mxs = max3f(rscat<0>(hi[0], hi[1], hi[2], hi[3]), Pt[0], Pt[1]) * -L2E;
    const float mns = min3f(rscat<1>(lo[0], lo[1], lo[2], lo[3]), Pt[0], Pt[1]) * L2E;
    float mx[4], mn[4];
    agath(mxs, mx); agath(mns, mn);
    float ep[4][4], en[4][4], sp[4], sn[4];
#pragma unroll
    for (int b = 0; b < 4; ++b) {
#pragma unroll
      for (int r = 0; r < 4; ++r) { ep[b][r] = __builtin_amdgcn_exp2f(__builtin_fmaf(P[b][r], L2E, mx[b])); en[b][r] = __builtin_amdgcn_exp2f(__builtin_fmaf(P[b][r], -L2E, mn[b])); }
      const f2 s1 = (f2){ep[b][0], ep[b][1]} + (f2){ep[b][2], ep[b][3]}, s2 = (f2){en[b][0], en[b][1]} + (f2){en[b][2], en[b][3]};
      sp[b] = s1[0] + s1[1]; sn[b] = s2[0] + s2[1];
    }
    float tp[2], tn[2];
#pragma unroll
    for (int c = 0; c < 2; ++c) { tp[c] = __builtin_amdgcn_exp2f(__builtin_fmaf(Pt[c], L2E, mxs)); tn[c] = __builtin_amdgcn_exp2f(__builtin_fmaf(Pt[c], -L2E, mns)); }
    const float ips = __builtin_amdgcn_rcpf(rscat<2>(sp[0], sp[1], sp[2], sp[3]) + (tp[0] + tp[1]));
    const float ins = __builtin_amdgcn_rcpf(rscat<2>(sn[0], sn[1], sn[2], sn[3]) + (tn[0] + tn[1]));
    float ip[4], in[4];
    agath(ips, ip); agath(ins, in);
    u4 om[4];
#pragma unroll
    for (int b = 0; b < 4; ++b) {
      om[b][0] = cvtpk((f2){ep[b][0], ep[b][1]} * ip[b]); om[b][1] = cvtpk((f2){ep[b][2], ep[b][3]} * ip[b]);
      om[b][2] = cvtpk((f2){en[b][0], en[b][1]} * in[b]); om[b][3] = cvtpk((f2){en[b][2], en[b][3]} * in[b]);
    }
    const u2 ot = {cvtpk((f2){tp[0], tp[1]} * ips), cvtpk((f2){tn[0], tn[1]} * ins)};
    const int tt = tt0 + mt;
    u4* qdst = (u4*)phiQ + ((size_t)tt * 32 + 2 * hp) * 64 + lane;
    AS3 char* row = stg + (mt * 16 + fi) * 160;
    if (odd) {
      qdst[0] = om[2]; qdst[64] = om[3];
      *(AS3 u4*)(row + fg * 16) = om[0]; *(AS3 u4*)(row + 5120 + fg * 16) = om[1];
    } else {
      qdst[0] = om[0]; qdst[64] = om[1];
      *(AS3 u4*)(row + fg * 16) = om[2]; *(AS3 u4*)(row + 5120 + fg * 16) = om[3];
    }
    if ((fg >> 1) == (hp & 1)) *(u2*)((char*)phiQt + (((size_t)tt * 4 + (hp >> 2)) * 64 + lane) * 16 + ((hp >> 1) & 1) * 8) = ot;
    else *(AS3 u2*)(row + (fg & 1) * 5120 + 64) = ot;
#pragma unroll
    for (int hh = 0; hh < 2; ++hh) {
      *(AS3 u2*)(row + hh * 5120 + 80 + fg * 8) = (u2){pk[4 + hh][0], pk[4 + hh][1]};
      if (fg == 2) *(AS3 u2*)(row + hh * 5120 + 112) = (u2){pk[6][hh], 0x00003f80u};
    }
  }
  ST(1, 3);
  {
    AS3 float* img = (AS3 float*)(smem + 40 * 1024) + w * 2 * KV_STRIDE;
    const int par = (hp >> 1) & 1;
#pragma unroll
    for (int hh = 0; hh < 2; ++hh) {
      const AS3 char* bk = stg + hh * 5120 + (4 * fg + (fi >> 2)) * 160 + (fi & 3) * 8;
      s8 bv[2];
#pragma unroll
      for (int nt = 0; nt < 2; ++nt) bv[nt] = __builtin_shufflevector(trread(bk + 80 + nt * 32), trread(bk + 80 + nt * 32 + 16 * 160), 0, 1, 2, 3, 4, 5, 6, 7);
      AS3 float* kp = img + hh * KV_STRIDE;
#pragma unroll
      for (int mt = 0; mt < 3; ++mt) {
        s8 a = __builtin_shufflevector(trread(bk + mt * 32), trread(bk + mt * 32 + 16 * 160), 0, 1, 2, 3, 4, 5, 6, 7);
        f4 k0 = mfma32(a, bv[0], z4), k1 = mfma32(a, bv[1], z4);
        if (mt < 2) {
          *(AS3 f4*)(kp + ((2 * mt + (fg >> 1)) * 16 + fi) * 8 + (fg & 1) * 4) = k0;
          if (fi < 3) *(AS3 f4*)(kp + 640 + fi * 40 + 16 * mt + 4 * fg) = k1;
        } else if (fg == 0) {
          *(AS3 f4*)(kp + 512 + fi * 8 + par * 4) = k0;
          if (fi < 3) *(AS3 f4*)(kp + 640 + fi * 40 + 32 + par * 4) = k1;
        }
      }
    }
  }
  ST(1, 4);
  BARRIER();
  {
    const AS3 float* kp = (const AS3 float*)(smem + 40 * 1024);
    float* gp = kvacc + 2 * hp * KV_STRIDE;
    const int par = (hp >> 1) & 1;
#pragma unroll
    for (int i = 0; i < 6; ++i) {
      const int a = i * 256 + tid, q = a % KV_STRIDE, t = (q - 640) % 40;
      const bool ok = q < 512 ? true : q < 640 ? ((q >> 2) & 1) == par : q < 760 && (t < 32 || ((t >> 2) & 1) == par);
      const float v = (kp[a] + kp[a + 2 * KV_STRIDE]) + (kp[a + 4 * KV_STRIDE] + kp[a + 6 * KV_STRIDE]);
      if (ok) unsafeAtomicAdd(gp + a, v);
    }
  }
  ST(1, 5);
  ST(1, 6);
  ST_OUT(1);
}

__global__ __launch_bounds__(704, 3) void kB(const bf16* __restrict__ phiQ, const bf16* __restrict__ phiQt, const float* __restrict__ kvacc,
                                             const bf16* __restrict__ woF, const bf16* __restrict__ xresF, bf16* __restrict__ xout, float* __restrict__ stats) {
  __shared__ __attribute__((aligned(16))) char smem[81 * 1024 + 32 * KV_STRIDE * 2 + 24 * 1024];
  const int tid = threadIdx.x, lane = tid & 63, w = tid >> 6;
  int bid = blockIdx.x;
  bid = (bid & 7) * 32 + (bid >> 3);
  const int rt = bid >> 3, ct = bid & 7;
  ST_DECL ST(2, 0);
  if (w >= 8) {
    const int lw = w - 8;
    const char* src = (const char*)woF + (size_t)ct * 81 * 1024 + lw * 3072 + lane * 16;
    char* dst0 = smem + lw * 3072;
#pragma unroll
    for (int i = 0; i < 9; ++i) {
      const int u = i < 8 ? (i >> 1) + (i & 1) * 4 : 8;
#pragma unroll
      for (int j = 0; j < 3; ++j) glds16(src + u * 9216 + j * 1024, dst0 + u * 9216 + j * 1024);
    }
#pragma unroll
    for (int u = 0; u < 5; ++u) asm volatile("s_waitcnt vmcnt(%0)\n\ts_barrier" :: "i"(u < 4 ? 21 - 6 * u : 0) : "memory");
    return;
  }
  const int fi = lane & 15, fg = lane >> 4;
  const int tt = rt * 4 + (w & 3), hf = w >> 2;
  AS3 char* kvl = (AS3 char*)smem + 81 * 1024;
  s8 pb[16], pt[2];
  {
    f4 a[6], b[6];
#pragma unroll
    for (int i = 0; i < 6; ++i) { const f4* p = (const f4*)(kvacc + (i * 512 + tid) * 8); a[i] = p[0]; b[i] = p[1]; }
    SB();
#pragma unroll
    for (int q = 0; q < 2; ++q) pt[q] = ((const s8*)phiQt)[((size_t)tt * 4 + hf * 2 + q) * 64 + lane];
    const s8* pp = (const s8*)phiQ + ((size_t)tt * 32 + hf * 16) * 64 + lane;
#pragma unroll
    for (int h = 0; h < 16; ++h) { pb[h] = pp[h * 64]; SB(); }
#pragma unroll
    for (int i = 0; i < 6; ++i) {
      u4 o = {cvtpk((f2){a[i][0], a[i][1]}), cvtpk((f2){a[i][2], a[i][3]}), cvtpk((f2){b[i][0], b[i][1]}), cvtpk((f2){b[i][2], b[i][3]})};
      *(AS3 u4*)(kvl + (i * 512 + tid) * 16) = o;
    }
  }
  int o1[4], o2[4];
#pragma unroll
  for (int m = 0; m < 4; ++m) {
    o1[m] = fg == m ? 1024 + fi * 16 : 1520;
    o2[m] = ((fi >> 2) == m && (fi & 3) < 3) ? 1280 + (fi & 3) * 80 + fg * 16 : 1520;
  }
  const int o3 = ((fi >> 2) == fg && (fi & 3) < 3) ? fg * (KV_STRIDE * 2) + 1280 + (fi & 3) * 80 + 64 : 1520;
  const AS3 s8* wl = (const AS3 s8*)smem + lane + hf * 4 * 576;
  const AS3 char* kvh = kvl + hf * 16 * (KV_STRIDE * 2);
  f4 acc[6];
#pragma unroll
  for (int a = 0; a < 6; ++a) acc[a] = (f4){0, 0, 0, 0};
  const f4 z4 = {0, 0, 0, 0};
  unsigned treg[4];
  BARRIER();
  ST(2, 1);
#pragma unroll
  for (int u = 0; u < 4; ++u) {
    if (u > 0) BAR0();
    f4 om[4], otl = z4;
#pragma unroll
    for (int m = 0; m < 4; ++m) {
      const int h = 4 * u + m;
      const AS3 char* kb = kvh + h * (KV_STRIDE * 2);
      s8 f0 = *(const AS3 s8*)(kb + lane * 16);
      s8 f1 = *(const AS3 s8*)(kb + o1[m]);
      s8 f2_ = *(const AS3 s8*)(kb + o2[m]);
      om[m] = mfma32(f0, pb[h], z4);
      otl = mfma32(f2_, pb[h], otl);
      om[m] = mfma32(f1, pt[u >> 1], om[m]);
    }
    otl = mfma32(*(const AS3 s8*)(kvh + 4 * u * (KV_STRIDE * 2) + o3), pt[u >> 1], otl);
    const float invs = __builtin_amdgcn_rcpf(otl[2] + 1e-6f);
    treg[u] = cvtpk((f2){otl[0], otl[1]} * invs);
    float inv[4];
    agath(invs, inv);
    unsigned po[4][2];
#pragma unroll
    for (int m = 0; m < 4; ++m) { po[m][0] = cvtpk((f2){om[m][0], om[m][1]} * inv[m]); po[m][1] = cvtpk((f2){om[m][2], om[m][3]} * inv[m]); }
    s8 ob[2];
    ob[0] = mk8(po[0][0], po[0][1], po[1][0], po[1][1]);
    ob[1] = mk8(po[2][0], po[2][1], po[3][0], po[3][1]);
    s8 wt = wl[(u * 9 + 8) * 64];
#pragma unroll
    for (int s = 0; s < 2; ++s) {
#pragma unroll
      for (int nt = 0; nt < 4; ++nt) acc[nt] = mfma32(wl[(u * 9 + 4 * s + nt) * 64], ob[s], acc[nt]);
      acc[4 + s] = mfma32(wt, ob[s], acc[4 + s]);
    }
  }
  BAR0();
  {
    const AS3 s8* w8 = (const AS3 s8*)smem + lane + 8 * 576;
    const s8 ob = mk8(treg[0], treg[1], treg[2], treg[3]);
#pragma unroll
    for (int nt = 0; nt < 4; ++nt) acc[nt] = mfma32(w8[(4 * hf + nt) * 64], ob, acc[nt]);
    f4 tl = mfma32(w8[8 * 64], ob, hf ? acc[5] : acc[4]);
    if (hf) acc[5] = tl; else acc[4] = tl;
  }
  ST(2, 2);
  {
    AS3 f4* rbuf = (AS3 f4*)(smem + 81 * 1024 + 32 * KV_STRIDE * 2) + (w & 3) * 6 * 64 + lane;
    if (hf) {
#pragma unroll
      for (int a = 0; a < 6; ++a) rbuf[a * 64] = acc[a];
    }
    BARRIER();
    if (hf) return;
#pragma unroll
    for (int a = 0; a < 6; ++a) acc[a] += rbuf[a * 64];
  }
  ST(2, 3);
#pragma unroll
  for (int r = 0; r < 4; ++r) acc[4][r] += __shfl(acc[5][r], lane + 32);
  const int tok = tt * 16 + fi;
  float v[5][4];
  float sum = 0.f;
#pragma unroll
  for (int nt = 0; nt < 5; ++nt) {
    const int col = ct * 72 + nt * 16 + fg * 4;
    const bool valid = nt < 4 || fg < 2;
    const int colc = valid ? col : ct * 72;
    u2 xr = *(const u2*)(xresF + xF_index(tok, colc));
#pragma unroll
    for (int r = 0; r < 4; ++r) {
      float xv = bfu((xr[r >> 1] >> ((r & 1) * 16)) & 0xffff);
      float o = rb(xv + rb(acc[nt][r]));
      v[nt][r] = o;
      sum += valid ? o : 0.f;
    }
    if (valid) {
      u2 o2_; o2_[0] = pack2(v[nt][0], v[nt][1]); o2_[1] = pack2(v[nt][2], v[nt][3]);
      *(u2*)(xout + xF_index(tok, col)) = o2_;
    }
  }
  sum = red_sum(sum);
  const float mean = sum * (1.0f / 72.0f);
  float m2 = 0.f;
#pragma unroll
  for (int nt = 0; nt < 5; ++nt)
#pragma unroll
    for (int r = 0; r < 4; ++r) { float d = v[nt][r] - mean; m2 += (nt < 4 || fg < 2) ? d * d : 0.f; }
  m2 = red_sum(m2);
  if (fg == 0) { float2 st; st.x = mean; st.y = m2; *(float2*)(stats + (tok * 8 + ct) * 2) = st; }
  ST(2, 4);
  ST_OUT(2);
}

DI void tr88(const u4 (&r)[8], u4 (&o)[8]) {
#pragma unroll
  for (int n = 0; n < 8; ++n)
#pragma unroll
    for (int j = 0; j < 4; ++j) o[n][j] = __builtin_amdgcn_perm(r[2 * j + 1][n >> 1], r[2 * j][n >> 1], (n & 1) ? 0x07060302u : 0x05040100u);
}
DI float sum8(u4 v) {
  f2 a = unpk(v[0]) + unpk(v[1]), b = unpk(v[2]) + unpk(v[3]);
  a += b;
  return a[0] + a[1];
}
typedef unsigned u4a __attribute__((ext_vector_type(4), aligned(4)));
#define PT 576
#define PB_QKV 32
#define PB_X 29
#define PB_PHI 13
#define PB_W1 108
#define PB_WO 18
#define PB_W2 144
#define PB_TOTAL (PB_QKV + PB_X + PB_PHI + PB_W1 + PB_WO + PB_W2)
#define PFS 65
__global__ __launch_bounds__(PT) void kP(const bf16* __restrict__ x0, const bf16* __restrict__ wqkv, const bf16* __restrict__ wphi, const bf16* __restrict__ wo,
                                          const bf16* __restrict__ w1, const bf16* __restrict__ b1, const bf16* __restrict__ w2,
                                          bf16* __restrict__ x0F, float* __restrict__ stats, bf16* __restrict__ wqkvF, float* __restrict__ csA,
                                          bf16* __restrict__ wphiF, bf16* __restrict__ woF, bf16* __restrict__ w1F, float* __restrict__ cs1,
                                          bf16* __restrict__ w2F, float* __restrict__ kvacc) {
  __shared__ float red[72 * 64];
  __shared__ u4 stg[72 * PFS];
  int b = blockIdx.x;
  const int t = threadIdx.x, lane = t & 63, w = t >> 6;
  u4 r[8], o[8];
  if (b < PB_QKV) {
    const int hp = b >> 1, hf = b & 1, no = hf ? 6 : 8;
    if (t < no * 72) {
      const int ol = t % no, kb = t / no, oc = hf * 8 + ol, j = oc >> 1, half = oc & 1;
      const bf16* src = wqkv + (size_t)kb * 8 * (3 * D_);
      if (j < 6) {
        const int jp = j < 4 ? j ^ (2 * (hp & 1)) : j;
        const int c0 = 576 * (jp >> 1) + 18 * (2 * hp + (jp & 1)) + 8 * half;
#pragma unroll
        for (int e = 0; e < 8; ++e) r[e] = *(const u4a*)(src + e * (3 * D_) + c0);
      } else {
#pragma unroll
        for (int e = 0; e < 8; ++e)
#pragma unroll
          for (int q = 0; q < 4; ++q) {
            const int tt = half == 0 ? q ^ (2 * (hp & 1)) : (4 * half + q) < 6 ? 4 * half + q : 0;
            unsigned v = *(const unsigned*)(src + e * (3 * D_) + 576 * (tt >> 1) + 18 * (2 * hp + (tt & 1)) + 16);
            r[e][q] = (4 * half + q) < 6 ? v : 0u;
          }
      }
      tr88(r, o);
      u4* dst = stg + ((kb >> 2) * 4 + (ol >> 1)) * PFS + (kb & 3) * 16 + half * 8;
#pragma unroll
      for (int n = 0; n < 8; ++n) { dst[n] = o[n]; red[kb * 64 + ol * 8 + n] = sum8(o[n]); }
    }
    __syncthreads();
    u4* dst = (u4*)wqkvF + ((size_t)hp * 18 * 7 + hf * 4) * 64 + lane;
#pragma unroll
    for (int f = 0; f < 8; ++f) {
      const int ks = w * 2 + (f >> 2), jl = f & 3;
      if (jl * 2 < no) dst[(ks * 7 + jl) * 64] = stg[(ks * 4 + jl) * PFS + lane];
    }
    if (t < no * 8) {
      float s0 = 0.f, s1 = 0.f, s2 = 0.f, s3 = 0.f;
#pragma unroll
      for (int k = 0; k < 72; k += 4) { s0 += red[k * 64 + t]; s1 += red[(k + 1) * 64 + t]; s2 += red[(k + 2) * 64 + t]; s3 += red[(k + 3) * 64 + t]; }
      csA[hp * 112 + hf * 64 + t] = (s0 + s1) + (s2 + s3);
    }
    return;
  }
  b -= PB_QKV;
  if (b < PB_X) {
    const int idx = b * PT + t;
    if (idx < 2 * 32 * KV_STRIDE / 4) ((f4*)kvacc)[idx] = (f4){0.f, 0.f, 0.f, 0.f};
    if (idx >= S_ * 8) return;
    const int tok = idx >> 3, seg = idx & 7;
    u4 v[9];
#pragma unroll
    for (int q = 0; q < 9; ++q) v[q] = *(const u4*)(x0 + tok * D_ + seg * 72 + q * 8);
    float s = 0.f;
#pragma unroll
    for (int q = 0; q < 9; ++q) {
      const int k = seg * 72 + q * 8;
      ((u4*)x0F)[((tok >> 4) * 18 + (k >> 5)) * 64 + ((k >> 3) & 3) * 16 + (tok & 15)] = v[q];
      s += sum8(v[q]);
    }
    const float m = s * (1.0f / 72.0f);
    f2 m2 = {0.f, 0.f};
#pragma unroll
    for (int q = 0; q < 9; ++q)
#pragma unroll
      for (int c = 0; c < 4; ++c) { f2 d = unpk(v[q][c]) - m; m2 += d * d; }
    float2 st; st.x = m; st.y = m2[0] + m2[1];
    *(float2*)(stats + idx * 2) = st;
    return;
  }
  b -= PB_X;
  if (b < PB_PHI) {
    const int idx = b * PT + t;
    if (idx >= 16 * 7 * 64) return;
    const int f = (idx >> 6) % 7, hp = idx / (7 * 64);
    const int i = lane & 15, g = lane >> 4;
    unsigned short val[8];
#pragma unroll
    for (int e = 0; e < 8; ++e) {
      int bb = 0, d = 0, ft = 0; bool ok = false;
      if (f < 4) {
        bb = f; ft = i;
        if (e < 4) { d = 4 * g + e; ok = true; }
        else { const int rho = 4 * g + e - 4; d = 16 + (rho & 1); ok = (rho >> 1) == bb; }
      } else {
        const int c = i & 3, sl = i >> 2;
        bb = sl; ft = 16 + (c & 1);
        if (f == 4) { d = 4 * g + (e & 3); ok = bb == (e >> 2); }
        else if (f == 5) { d = 4 * g + (e & 3); ok = bb == 2 + (e >> 2); }
        else { const int rho = 4 * g + e; d = 16 + (rho & 1); ok = e < 4 && (rho >> 1) == bb; }
        ok = ok && c < 2;
      }
      unsigned short wv = __builtin_bit_cast(unsigned short, wphi[((2 * hp + (bb & 1)) * DH_ + d) * DH_ + ft]);
      val[e] = ok ? wv : (unsigned short)0;
    }
    u4 ov;
#pragma unroll
    for (int e = 0; e < 4; ++e) ov[e] = (unsigned)val[2 * e] | ((unsigned)val[2 * e + 1] << 16);
    ((u4*)wphiF)[idx] = ov;
    return;
  }
  b -= PB_PHI;
  if (b < PB_W1) {
    const int cb = b / 3, kc = b % 3;
    const int oc = t & 15, kbl = t >> 4, kb = kc * 24 + kbl, n0 = cb * 128 + oc * 8;
    if (t >= 384) return;
    const bf16* src = w1 + (size_t)kb * 8 * F_ + n0;
#pragma unroll
    for (int e = 0; e < 8; ++e) r[e] = *(const u4*)(src + e * F_);
    tr88(r, o);
    u4* dst = stg + ((kbl >> 2) * 8 + (oc >> 1)) * PFS + (kbl & 3) * 16 + (oc & 1) * 8;
#pragma unroll
    for (int n = 0; n < 8; ++n) { dst[n] = o[n]; red[kbl * 128 + oc * 8 + n] = sum8(o[n]); }
    __syncthreads();
#pragma unroll
    for (int f = 0; f < 8; ++f) {
      const int T = cb * 8 + f;
      ((u4*)w1F)[(((size_t)(T / 9) * 18 + kc * 6 + w) * 9 + T % 9) * 64 + lane] = stg[(w * 8 + f) * PFS + lane];
    }
    if (t < 128) {
      float s = 0.f;
#pragma unroll
      for (int k = 0; k < 24; ++k) s += red[k * 128 + t];
      cs1[kc * F_ + cb * 128 + t] = s;
      if (kc == 0) cs1[3 * F_ + cb * 128 + t] = b2f(b1[cb * 128 + t]);
    }
    return;
  }
  b -= PB_W1;
  const bool isWo = b < PB_WO;
  if (!isWo) b -= PB_WO;
  const int kp = b >> 1, ch = b & 1;
  if (t >= 384) return;
  if (t < 288) {
    const int oc = t % 36, kbl = t / 36, ks = kp * 2 + (kbl >> 2), g = kbl & 3;
    const bf16* src = (isWo ? wo : w2) + ch * 288 + oc * 8;
#pragma unroll
    for (int e = 0; e < 8; ++e) {
      const int row = isWo ? wo_k(ks, g, e) : 32 * ks + (e < 4 ? 4 * g + e : 12 + 4 * g + e);
      r[e] = *(const u4*)(src + (size_t)row * D_);
    }
    tr88(r, o);
    const int q = oc % 9;
    const int u = q < 8 ? (ks & 1) * 4 + (q >> 1) : 8, i0 = q < 8 ? (q & 1) * 8 : (ks & 1) * 8;
    u4* dst = stg + ((oc / 9) * 9 + u) * PFS + g * 16 + i0;
#pragma unroll
    for (int n = 0; n < 8; ++n) dst[n] = o[n];
  }
  __syncthreads();
#pragma unroll
  for (int f = 0; f < 6; ++f) {
    const int fr = w * 6 + f, ct = ch * 4 + fr / 9;
    ((u4*)(isWo ? woF : w2F))[(((size_t)ct * (isWo ? 9 : 72) + kp) * 9 + fr % 9) * 64 + lane] = stg[fr * PFS + lane];
  }
}

extern "C" void kernel_launch(void* const* d_in, const int* in_sizes, int n_in,
                              void* d_out, int out_size, void* d_ws, size_t ws_size,
                              hipStream_t stream) {
  const bf16* x0 = (const bf16*)d_in[0];
  const bf16* wqkv = (const bf16*)d_in[1];
  const bf16* wphi = (const bf16*)d_in[2];
  const bf16* wo = (const bf16*)d_in[3];
  const bf16* w1 = (const bf16*)d_in[5];
  const bf16* b1 = (const bf16*)d_in[6];
  const bf16* w2 = (const bf16*)d_in[7];
  const bf16* b2 = (const bf16*)d_in[8];
  char* ws = (char*)d_ws;
  size_t off = 0;
  auto alloc = [&](size_t bytes) { void* p = ws + off; off += (bytes + 255) & ~(size_t)255; return p; };
  bf16* x0F = (bf16*)alloc(S_ * D_ * 2);
  bf16* x1F = (bf16*)alloc(S_ * D_ * 2);
  bf16* x2F = (bf16*)alloc(S_ * D_ * 2);
  bf16* h = (bf16*)alloc((size_t)S_ * F_ * 2);
  bf16* w2F = (bf16*)alloc((size_t)8 * 72 * 9 * 1024);
  bf16* w1F = (bf16*)alloc((size_t)D_ * F_ * 2);
  bf16* wqkvF = (bf16*)alloc((size_t)16 * 126 * 1024 + 2048);
  bf16* woF = (bf16*)alloc((size_t)8 * 81 * 1024);
  bf16* wphiF = (bf16*)alloc((size_t)16 * 7 * 1024);
  bf16* phiQ = (bf16*)alloc((size_t)128 * 32 * 1024);
  bf16* phiQt = (bf16*)alloc((size_t)128 * 8 * 512);
  float* kvacc = (float*)alloc(2 * 32 * KV_STRIDE * 4);
  float* cs1 = (float*)alloc(F_ * 16);
  float* csA = (float*)alloc(3 * 16 * 112 * 4);
  float* stats1 = (float*)alloc(S_ * 16 * 4 + 65536);
  float* stats = (float*)alloc(S_ * 16 * 4 + 65536);
  kP<<<PB_TOTAL, PT, 0, stream>>>(x0, wqkv, wphi, wo, w1, b1, w2, x0F, stats, wqkvF, csA, wphiF, woF, w1F, cs1, w2F, kvacc);
  const bf16* xin = x0F;
  for (int layer = 0; layer < 2; ++layer) {
    float* kv = kvacc + layer * 32 * KV_STRIDE;
    kA<<<256, 512, 0, stream>>>(xin, wqkvF, csA, wphiF, stats, phiQ, phiQt, kv);
    kB<<<256, 704, 0, stream>>>(phiQ, phiQt, kv, woF, xin, x1F, stats1);
    kC<<<256, 704, 0, stream>>>(x1F, w1F, cs1, stats1, h);
    if (layer == 0) kD<false><<<256, 448, 0, stream>>>(h, w2F, b2, x1F, x2F, stats);
    else kD<true><<<256, 448, 0, stream>>>(h, w2F, b2, x1F, (bf16*)d_out, stats);
    xin = x2F;
  }
}
```

```cpp
#include <hip/hip_runtime.h>
#include <hip/hip_bf16.h>
#include <stdint.h>

typedef __hip_bfloat16 bf16;
typedef short s4 __attribute__((ext_vector_type(4)));
typedef short s8 __attribute__((ext_vector_type(8)));
typedef float f4 __attribute__((ext_vector_type(4)));
typedef __bf16 bf8 __attribute__((ext_vector_type(8)));
typedef unsigned u4 __attribute__((ext_vector_type(4)));
typedef unsigned u2 __attribute__((ext_vector_type(2)));
#define AS3 __attribute__((address_space(3)))
#define AS1 __attribute__((address_space(1)))
#define DI __device__ __forceinline__

#define S_ 2048
#define D_ 576
#define H_ 32
#define DH_ 18
#define F_ 4608

DI float b2f(bf16 x) { return __bfloat162float(x); }
DI bf16 f2b(float x) { return __float2bfloat16(x); }
DI float rb(float x) { return b2f(f2b(x)); }
DI float bfu(unsigned short u) { return __uint_as_float(((unsigned)u) << 16); }
DI unsigned pack2(float a, float b) {
  bf16 x = f2b(a), y = f2b(b);
  return (unsigned)__builtin_bit_cast(unsigned short, x) | ((unsigned)__builtin_bit_cast(unsigned short, y) << 16);
}
typedef float f2 __attribute__((ext_vector_type(2)));
typedef short s2 __attribute__((ext_vector_type(2)));
typedef __bf16 bf2 __attribute__((ext_vector_type(2)));
DI unsigned cvtpk(f2 v) { return __builtin_bit_cast(unsigned, __builtin_convertvector(v, bf2)); }
DI f2 unpk(unsigned u) { f2 r; r[0] = __uint_as_float(u << 16); r[1] = __uint_as_float(u & 0xffff0000u); return r; }
DI f4 mfma32(s8 a, s8 b, f4 c) { return __builtin_amdgcn_mfma_f32_16x16x32_bf16(__builtin_bit_cast(bf8, a), __builtin_bit_cast(bf8, b), c, 0, 0, 0); }
DI void mfma_acc(f4& c, s8 a, s8 b) { asm volatile("v_mfma_f32_16x16x32_bf16 %0, %1, %2, %0" : "+v"(c) : "v"(a), "v"(b)); }
#define MFMA_FENCE() asm volatile("s_nop 7\n\ts_nop 3" ::: "memory")
#define LDS_R128(d, addr, off) asm volatile("ds_read_b128 %0, %1 offset:%2" : "=v"(d) : "v"(addr), "i"(off))
#define LDS_RTR(d, addr, off) asm volatile("ds_read_b64_tr_b16 %0, %1 offset:%2" : "=v"(d) : "v"(addr), "i"(off))
DI unsigned lds_addr(const void* p) { return (unsigned)(uintptr_t)(AS3 const char*)p; }
DI const AS3 s8* opaque_lds(const AS3 s8* p) { unsigned a = (unsigned)(uintptr_t)p; asm volatile("" : "+v"(a)); return (const AS3 s8*)(uintptr_t)a; }
DI void glds16(const void* g, char* l) { __builtin_amdgcn_global_load_lds((const AS1 unsigned*)g, (AS3 unsigned*)l, 16, 0, 0); }
DI void glds16a(const void* g, unsigned l) { asm volatile("s_mov_b32 m0, %1\n\ts_nop 0\n\tglobal_load_lds_dwordx4 %0, off" :: "v"(g), "s"(l) : "memory"); }
#define WAIT_V(n) asm volatile("s_waitcnt vmcnt(" #n ")" ::: "memory")

#define ST_DECL
#define ST(K, i)
#define CY0(K)
#define CY1(K)
#define ST_OUT(K)
DI size_t hF_index(int t, int f) {
  int ks = f >> 5, r = f & 31, hi = r >> 4, g = (r & 15) >> 2, e = (r & 3) | (hi << 2);
  return ((((size_t)(t >> 4) * (F_ / 32) + ks) * 64 + g * 16 + (t & 15)) << 3) + e;
}
DI size_t xF_index(int t, int k) { return ((((size_t)(t >> 4) * 18 + (k >> 5)) * 64 + ((k >> 3) & 3) * 16 + (t & 15)) << 3) + (k & 7); }
#define SB() __builtin_amdgcn_sched_barrier(0)
#define BAR0() asm volatile("s_barrier" ::: "memory")
#define BARRIER() asm volatile("s_waitcnt lgkmcnt(0)\n\ts_barrier" ::: "memory")

DI void ln_comb(const f4 (&p)[4], float& mean, float& rstd);
DI void ln_stats(const float* __restrict__ stats, int tok, float& mean, float& rstd) {
  const f4* p = (const f4*)(stats + tok * 16);
  f4 q[4] = {p[0], p[1], p[2], p[3]};
  ln_comb(q, mean, rstd);
}
DI void ln_comb(const f4 (&p)[4], float& mean, float& rstd) {
  f4 a = p[0], b = p[1], c = p[2], d = p[3];
  float m = (a[0] + a[2] + b[0] + b[2] + c[0] + c[2] + d[0] + d[2]) * 0.125f;
  float m2 = a[1] + a[3] + b[1] + b[3] + c[1] + c[3] + d[1] + d[3];
  float q = 0.f, t;
  t = a[0] - m; q += t * t; t = a[2] - m; q += t * t; t = b[0] - m; q += t * t; t = b[2] - m; q += t * t;
  t = c[0] - m; q += t * t; t = c[2] - m; q += t * t; t = d[0] - m; q += t * t; t = d[2] - m; q += t * t;
  float var = (m2 + 72.f * q) * (1.0f / 576.0f);
  mean = m; rstd = 1.0f / sqrtf(var + 1e-5f);
}

DI void ln_comb_var(const f4 (&p)[4], float& mean, float& var) {
  f4 a = p[0], b = p[1], c = p[2], d = p[3];
  float m = ((a[0] + a[2]) + (b[0] + b[2]) + ((c[0] + c[2]) + (d[0] + d[2]))) * 0.125f;
  float m2 = (a[1] + a[3]) + (b[1] + b[3]) + ((c[1] + c[3]) + (d[1] + d[3]));
  float q = 0.f, t;
  t = a[0] - m; q += t * t; t = a[2] - m; q += t * t; t = b[0] - m; q += t * t; t = b[2] - m; q += t * t;
  t = c[0] - m; q += t * t; t = c[2] - m; q += t * t; t = d[0] - m; q += t * t; t = d[2] - m; q += t * t;
  mean = m; var = (m2 + 72.f * q) * (1.0f / 576.0f) + 1e-5f;
}
DI void ln_comb2(f4 a, float& mean, float& rstd) {
  const float m = (a[0] + a[2]) * 0.5f, dlt = a[0] - a[2];
  const float var = (a[1] + a[3] + 144.f * dlt * dlt) * (1.0f / 576.0f);
  mean = m; rstd = 1.0f / sqrtf(var + 1e-5f);
}
DI int qkv_col(int hp, int j, int i) {
  if (j < 6) return 576 * (j >> 1) + 18 * (2 * hp + (j & 1)) + i;
  if (i >= 12) return -1;
  int t = i >> 1;
  return 576 * (t >> 1) + 18 * (2 * hp + (t & 1)) + 16 + (i & 1);
}
DI int wo_k(int s, int g, int e) {
  if (s < 16) return 18 * (2 * s + (e >> 2)) + 4 * g + (e & 3);
  int q = 4 * (s - 16) + (e >> 1);
  return 18 * (4 * q + g) + 16 + (e & 1);
}
DI float red_max(float x) {
  auto a = __builtin_amdgcn_permlane16_swap(__float_as_uint(x), __float_as_uint(x), false, false);
  x = fmaxf(__uint_as_float(a[0]), __uint_as_float(a[1]));
  auto b = __builtin_amdgcn_permlane32_swap(__float_as_uint(x), __float_as_uint(x), false, false);
  return fmaxf(__uint_as_float(b[0]), __uint_as_float(b[1]));
}
DI float red_min(float x) {
  auto a = __builtin_amdgcn_permlane16_swap(__float_as_uint(x), __float_as_uint(x), false, false);
  x = fminf(__uint_as_float(a[0]), __uint_as_float(a[1]));
  auto b = __builtin_amdgcn_permlane32_swap(__float_as_uint(x), __float_as_uint(x), false, false);
  return fminf(__uint_as_float(b[0]), __uint_as_float(b[1]));
}
DI float red_sum(float x) {
  auto a = __builtin_amdgcn_permlane16_swap(__float_as_uint(x), __float_as_uint(x), false, false);
  x = __uint_as_float(a[0]) + __uint_as_float(a[1]);
  auto b = __builtin_amdgcn_permlane32_swap(__float_as_uint(x), __float_as_uint(x), false, false);
  return __uint_as_float(b[0]) + __uint_as_float(b[1]);
}
DI s8 mk8(unsigned a, unsigned b, unsigned c, unsigned d) { u4 v = {a, b, c, d}; return __builtin_bit_cast(s8, v); }
DI s8 zx8(s4 a) { return __builtin_shufflevector(a, (s4){0, 0, 0, 0}, 0, 1, 2, 3, 4, 5, 6, 7); }
DI s4 trread(const AS3 char* p) { return __builtin_amdgcn_ds_read_tr16_b64_v4i16((AS3 s4*)p); }
#define KV_STRIDE 768

DI float max2f(float a, float b) { return __builtin_elementwise_maximum(a, b); }
DI float min2f(float a, float b) { return __builtin_elementwise_minimum(a, b); }
DI float max3f(float a, float b, float c) { return max2f(max2f(a, b), c); }
DI float min3f(float a, float b, float c) { return min2f(min2f(a, b), c); }
DI void swap16(float& a, float& b) { auto r = __builtin_amdgcn_permlane16_swap(__float_as_uint(a), __float_as_uint(b), false, false); a = __uint_as_float(r[0]); b = __uint_as_float(r[1]); }
DI void swap32(float& a, float& b) { auto r = __builtin_amdgcn_permlane32_swap(__float_as_uint(a), __float_as_uint(b), false, false); a = __uint_as_float(r[0]); b = __uint_as_float(r[1]); }
template <int OP> DI float op2(float a, float b) { return OP == 0 ? max2f(a, b) : OP == 1 ? min2f(a, b) : a + b; }
template <int OP> DI float rscat(float a, float b, float c, float d) {
  swap16(a, b); swap16(c, d);
  float x = op2<OP>(a, b), y = op2<OP>(c, d);
  swap32(x, y);
  return op2<OP>(x, y);
}
DI void agath(float r, float (&o)[4]) {
  float x = r, y = r;
  swap16(x, y);
  float x2 = x, y2 = y;
  swap32(x, x2); swap32(y, y2);
  o[0] = x; o[2] = x2; o[1] = y; o[3] = y2;
}
#define A_XW 6
#define A_GD 3
__global__ __launch_bounds__(512, 2) void kA(const bf16* __restrict__ xF, const bf16* __restrict__ wqkvF, const float* __restrict__ csA,
                                             const bf16* __restrict__ wphiF, const float* __restrict__ stats,
                                             bf16* __restrict__ phiQ, bf16* __restrict__ phiQt, float* __restrict__ kvacc) {
  __shared__ __attribute__((aligned(16))) char smem[128 * 1024];
  const int tid = threadIdx.x, lane = tid & 63, w = tid >> 6;
  const int xcd = blockIdx.x & 7, li = blockIdx.x >> 3;
  const int rt = (xcd & 3) * 4 + (li & 3), hp = (xcd >> 2) * 8 + (li >> 2);
  ST_DECL ST(1, 0);
  if (w >= 4) {
    const int lw = w - 4;
    const char* src = (const char*)wqkvF + (size_t)hp * 126 * 1024 + lw * 1024 + lane * 16;
    char* dst = smem + lw * 1024;
    auto cum = [](int g) { return g >= 8 ? 32 : (14 * (g + 1) + 3) / 4; };
#pragma unroll
    for (int i = 0; i < cum(A_GD - 1); ++i) glds16(src + i * 4096, dst + i * 4096);
#pragma unroll
    for (int g = 0; g < 9; ++g) {
      asm volatile("s_waitcnt vmcnt(%0)\n\ts_barrier" :: "i"(cum(g + A_GD - 1) - cum(g)) : "memory");
#pragma unroll
      for (int i = cum(g + A_GD - 1); i < cum(g + A_GD); ++i) glds16(src + i * 4096, dst + i * 4096);
    }
    return;
  }
  const int fi = lane & 15, fg = lane >> 4;
  const int tt0 = rt * 8 + w * 2;
  const s8* xp = (const s8*)xF + (size_t)tt0 * 18 * 64 + lane;
  const AS3 s8* wl = (const AS3 s8*)smem + lane;
  const AS3 s8* wl2 = opaque_lds(wl + 4096);
  auto wrd = [&](int piece) { return piece < 64 ? wl[piece * 64] : wl2[(piece - 64) * 64]; };
  float mean[2], rstd[2];
  f4 acc[7][2];
  s8 xb[18][2];
  {
    f4 st[2], cs[7];
#pragma unroll
    for (int mt = 0; mt < 2; ++mt) st[mt] = *(const f4*)(stats + ((tt0 + mt) * 16 + fi) * 4);
#pragma unroll
    for (int j = 0; j < 7; ++j) cs[j] = *(const f4*)(csA + hp * 112 + j * 16 + fg * 4);
    SB();
#pragma unroll
    for (int p = 0; p < A_XW; ++p) { xb[p][0] = xp[p * 64]; xb[p][1] = xp[(18 + p) * 64]; SB(); }
#pragma unroll
    for (int mt = 0; mt < 2; ++mt) ln_comb2(st[mt], mean[mt], rstd[mt]);
#pragma unroll
    for (int j = 0; j < 7; ++j) { acc[j][0] = cs[j] * (-mean[0]); acc[j][1] = cs[j] * (-mean[1]); }
    SB();
  }
  s8 wf[7], wp[7];
  BAR0();
  ST(1, 1); CY0(1);
#pragma unroll
  for (int j = 0; j < 7; ++j) wf[j] = wl[j * 64];
  SB();
#pragma unroll
  for (int u = 0; u < 18; ++u) {
    const bool rd = u + 1 < 18;
    if (rd && (u & 1)) BAR0();
#pragma unroll
    for (int j = 0; j < 7; ++j) {
      mfma_acc(acc[j][0], wf[j], xb[u][0]);
      SB();
      if (j > 0) { if (rd) wf[j - 1] = wrd((u + 1) * 7 + j - 1); }
      else if (u > 0) wf[6] = wrd(u * 7 + 6);
      SB();
      mfma_acc(acc[j][1], wf[j], xb[u][1]);
      SB();
    }
    if (u + A_XW < 18) { xb[u + A_XW][0] = xp[(u + A_XW) * 64]; xb[u + A_XW][1] = xp[(18 + u + A_XW) * 64]; SB(); }
    if (u == 10) {
#pragma unroll
      for (int f = 0; f < 7; ++f) wp[f] = ((const s8*)wphiF)[(hp * 7 + f) * 64 + lane];
      SB();
    }
  }
  MFMA_FENCE();
  SB();
  ST(1, 2); CY1(1);
  const f4 z4 = {0, 0, 0, 0};
  const float L2E = 1.4426950408889634f;
  AS3 char* stg = (AS3 char*)smem + w * 10240;
  const bool odd = hp & 1;
#pragma unroll
  for (int mt = 0; mt < 2; ++mt) {
    unsigned pk[7][2];
#pragma unroll
    for (int j = 0; j < 7; ++j)
#pragma unroll
      for (int r = 0; r < 2; ++r) pk[j][r] = cvtpk((f2){acc[j][mt][2 * r], acc[j][mt][2 * r + 1]} * rstd[mt]);
    f4 P[4], Pt;
#pragma unroll
    for (int b = 0; b < 4; ++b) P[b] = mfma32(wp[b], mk8(pk[b][0], pk[b][1], pk[6][0], pk[6][1]), z4);
    Pt = mfma32(wp[4], mk8(pk[0][0], pk[0][1], pk[1][0], pk[1][1]), z4);
    Pt = mfma32(wp[5], mk8(pk[2][0], pk[2][1], pk[3][0], pk[3][1]), Pt);
    Pt = mfma32(wp[6], mk8(pk[6][0], pk[6][1], pk[6][0], pk[6][1]), Pt);
    float hi[4], lo[4];
#pragma unroll
    for (int b = 0; b < 4; ++b) { hi[b] = max2f(max3f(P[b][0], P[b][1], P[b][2]), P[b][3]); lo[b] = min2f(min3f(P[b][0], P[b][1], P[b][2]), P[b][3]); }
    const float mxs = max3f(rscat<0>(hi[0], hi[1], hi[2], hi[3]), Pt[0], Pt[1]) * -L2E;
    const float mns = min3f(rscat<1>(lo[0], lo[1], lo[2], lo[3]), Pt[0], Pt[1]) * L2E;
    float mx[4], mn[4];
    agath(mxs, mx); agath(mns, mn);
    float ep[4][4], en[4][4], sp[4], sn[4];
#pragma unroll
    for (int b = 0; b < 4; ++b) {
#pragma unroll
      for (int r = 0; r < 4; ++r) { ep[b][r] = __builtin_amdgcn_exp2f(__builtin_fmaf(P[b][r], L2E, mx[b])); en[b][r] = __builtin_amdgcn_exp2f(__builtin_fmaf(P[b][r], -L2E, mn[b])); }
      const f2 s1 = (f2){ep[b][0], ep[b][1]} + (f2){ep[b][2], ep[b][3]}, s2 = (f2){en[b][0], en[b][1]} + (f2){en[b][2], en[b][3]};
      sp[b] = s1[0] + s1[1]; sn[b] = s2[0] + s2[1];
    }
    float tp[2], tn[2];
#pragma unroll
    for (int c = 0; c < 2; ++c) { tp[c] = __builtin_amdgcn_exp2f(__builtin_fmaf(Pt[c], L2E, mxs)); tn[c] = __builtin_amdgcn_exp2f(__builtin_fmaf(Pt[c], -L2E, mns)); }
    const float ips = __builtin_amdgcn_rcpf(rscat<2>(sp[0], sp[1], sp[2], sp[3]) + (tp[0] + tp[1]));
    const float ins = __builtin_amdgcn_rcpf(rscat<2>(sn[0], sn[1], sn[2], sn[3]) + (tn[0] + tn[1]));
    float ip[4], in[4];
    agath(ips, ip); agath(ins, in);
    u4 om[4];
#pragma unroll
    for (int b = 0; b < 4; ++b) {
      om[b][0] = cvtpk((f2){ep[b][0], ep[b][1]} * ip[b]); om[b][1] = cvtpk((f2){ep[b][2], ep[b][3]} * ip[b]);
      om[b][2] = cvtpk((f2){en[b][0], en[b][1]} * in[b]); om[b][3] = cvtpk((f2){en[b][2], en[b][3]} * in[b]);
    }
    const u2 ot = {cvtpk((f2){tp[0], tp[1]} * ips), cvtpk((f2){tn[0], tn[1]} * ins)};
    const int tt = tt0 + mt;
    u4* qdst = (u4*)phiQ + ((size_t)tt * 32 + 2 * hp) * 64 + lane;
    AS3 char* row = stg + (mt * 16 + fi) * 160;
    if (odd) {
      qdst[0] = om[2]; qdst[64] = om[3];
      *(AS3 u4*)(row + fg * 16) = om[0]; *(AS3 u4*)(row + 5120 + fg * 16) = om[1];
    } else {
      qdst[0] = om[0]; qdst[64] = om[1];
      *(AS3 u4*)(row + fg * 16) = om[2]; *(AS3 u4*)(row + 5120 + fg * 16) = om[3];
    }
    if ((fg >> 1) == (hp & 1)) *(u2*)((char*)phiQt + (((size_t)tt * 4 + (hp >> 2)) * 64 + lane) * 16 + ((hp >> 1) & 1) * 8) = ot;
    else *(AS3 u2*)(row + (fg & 1) * 5120 + 64) = ot;
#pragma unroll
    for (int hh = 0; hh < 2; ++hh) {
      *(AS3 u2*)(row + hh * 5120 + 80 + fg * 8) = (u2){pk[4 + hh][0], pk[4 + hh][1]};
      if (fg == 2) *(AS3 u2*)(row + hh * 5120 + 112) = (u2){pk[6][hh], 0x00003f80u};
    }
  }
  ST(1, 3);
  {
    AS3 float* img = (AS3 float*)(smem + 40 * 1024) + w * 2 * KV_STRIDE;
    const int par = (hp >> 1) & 1;
#pragma unroll
    for (int hh = 0; hh < 2; ++hh) {
      const AS3 char* bk = stg + hh * 5120 + (4 * fg + (fi >> 2)) * 160 + (fi & 3) * 8;
      s8 bv[2];
#pragma unroll
      for (int nt = 0; nt < 2; ++nt) bv[nt] = __builtin_shufflevector(trread(bk + 80 + nt * 32), trread(bk + 80 + nt * 32 + 16 * 160), 0, 1, 2, 3, 4, 5, 6, 7);
      AS3 float* kp = img + hh * KV_STRIDE;
#pragma unroll
      for (int mt = 0; mt < 3; ++mt) {
        s8 a = __builtin_shufflevector(trread(bk + mt * 32), trread(bk + mt * 32 + 16 * 160), 0, 1, 2, 3, 4, 5, 6, 7);
        f4 k0 = mfma32(a, bv[0], z4), k1 = mfma32(a, bv[1], z4);
        if (mt < 2) {
          *(AS3 f4*)(kp + ((2 * mt + (fg >> 1)) * 16 + fi) * 8 + (fg & 1) * 4) = k0;
          if (fi < 3) *(AS3 f4*)(kp + 640 + fi * 40 + 16 * mt + 4 * fg) = k1;
        } else if (fg == 0) {
          *(AS3 f4*)(kp + 512 + fi * 8 + par * 4) = k0;
          if (fi < 3) *(AS3 f4*)(kp + 640 + fi * 40 + 32 + par * 4) = k1;
        }
      }
    }
  }
  ST(1, 4);
  BARRIER();
  {
    const AS3 float* kp = (const AS3 float*)(smem + 40 * 1024);
    float* gp = kvacc + 2 * hp * KV_STRIDE;
    const int par = (hp >> 1) & 1;
#pragma unroll
    for (int i = 0; i < 6; ++i) {
      const int a = i * 256 + tid, q = a % KV_STRIDE, t = (q - 640) % 40;
      const bool ok = q < 512 ? true : q < 640 ? ((q >> 2) & 1) == par : q < 760 && (t < 32 || ((t >> 2) & 1) == par);
      const float v = (kp[a] + kp[a + 2 * KV_STRIDE]) + (kp[a + 4 * KV_STRIDE] + kp[a + 6 * KV_STRIDE]);
      if (ok) unsafeAtomicAdd(gp + a, v);
    }
  }
  ST(1, 5);
  ST(1, 6);
  ST_OUT(1);
}

DI float smul(float a, float b) { float r = a * b; asm("" : "+v"(r)); return r; }
DI void st_wt(void* p, u4 v) { asm volatile("global_store_dwordx4 %0, %1, off sc1\n\ts_nop 1" :: "v"(p), "v"(v) : "memory"); }
DI void st_wt(void* p, u2 v) { asm volatile("global_store_dwordx2 %0, %1, off sc1\n\ts_nop 1" :: "v"(p), "v"(v) : "memory"); }
typedef _Float16 h2 __attribute__((ext_vector_type(2)));
DI unsigned cvtph(f2 v) { return __builtin_bit_cast(unsigned, __builtin_convertvector(v, h2)); }
DI f2 unph(unsigned u) { return __builtin_convertvector(__builtin_bit_cast(h2, u), f2); }

#ifndef M_PD
#define M_PD 4
#endif
#ifndef M_PRIO
#define M_PRIO 2
#endif
#ifndef M_NS
#define M_NS 2
#endif
#ifndef M_WARM
#define M_WARM 0
#endif
#ifndef M_ST
#define M_ST 0
#endif
#ifndef M_P0
#define M_P0 1
#define M_P1 2
#endif
#ifndef M_X
#define M_X 0
#endif
#ifndef M_FLAG
#define M_FLAG 1
#endif
DI void lds_wait_ge(unsigned addr, unsigned n) {
  unsigned v, sv;
  asm volatile("LW%=:\n\tds_read_b32 %0, %2\n\ts_waitcnt lgkmcnt(0)\n\tv_readfirstlane_b32 %1, %0\n\ts_cmp_ge_u32 %1, %3\n\ts_cbranch_scc1 LD%=\n\ts_sleep 1\n\ts_branch LW%=\nLD%=:"
               : "=&v"(v), "=&s"(sv) : "v"(addr), "i"(n) : "memory", "scc");
}
#define M_CB (2 * 73728)
#define M_SS (M_CB + 5120)
#define M_FL (M_SS + 4096)
#ifndef M_TCH
#define M_TCH 0
#endif
DI void mfma_z(f4& c, s8 a, s8 b) { asm volatile("v_mfma_f32_16x16x32_bf16 %0, %1, %2, 0" : "=v"(c) : "v"(a), "v"(b)); }
DI float sfma(float a, float b, float c) { float r = __builtin_fmaf(a, b, c); asm("" : "+v"(r)); return r; }
__global__ __launch_bounds__(768, 3) void kM(const bf16* __restrict__ xF, const bf16* __restrict__ wM, const float* __restrict__ cb1,
                                             const float* __restrict__ stats, unsigned* __restrict__ part) {
  __shared__ __attribute__((aligned(16))) char smem[M_FL + 64];
  const int tid = threadIdx.x, lane = tid & 63, w = __builtin_amdgcn_readfirstlane(tid >> 6);
  const int xcd = blockIdx.x & 7, li = blockIdx.x >> 3;
  const int slice = (xcd % (8 / M_NS)) * M_NS + li % M_NS, rt = (xcd / (8 / M_NS)) * (32 / M_NS) + li / M_NS;
  const int fi = lane & 15, fg = lane >> 4;
  const unsigned l16 = lane * 16;
  ST_DECL ST(3, 0); ST(4, 0);
  if (w < 9) {
    const char* src = w < 5 ? (const char*)cb1 + slice * 4608 + w * 1024 : (const char*)stats + rt * 4096 + (w - 5) * 1024;
    glds16a(src + l16, lds_addr(smem) + M_CB + w * 1024);
  } else if (w == 9) ((AS3 unsigned*)(smem + M_FL))[lane & 15] = 0u;
  SB();
  const char* xsrc = (const char*)xF + (size_t)rt * 72 * 1024 + l16;
  auto xdma = [&](int i) { const int p = i * 12 + w; glds16a(xsrc + ((p & 3) * 18 + (p >> 2)) * 1024, lds_addr(smem) + p * 1024); };
  xdma(0); xdma(1);
  ST(4, 1);
  BAR0();
  SB();
  ST(4, 2);
  const char* wbase = (const char*)wM + (size_t)(slice * 12 + w) * 108 * 1024;
  auto wld = [&](int u, int t) { return *(const s8*)(wbase + (u * 3 + t) * 1024 + l16); };
  constexpr int TG = 9 * M_NS;
  const unsigned toff = ((li / M_NS) * TG + (lane < TG ? lane : 0)) * 64;
  unsigned tch = 0;
  auto touch = [&](int c) { if (M_TCH) { asm volatile("" :: "v"(tch)); tch = *(const unsigned*)(wbase + c * 18432 + toff); SB(); } };
  s8 wb[M_PD][3];
#pragma unroll
  for (int t = 0; t < 3; ++t) { wb[0][t] = wld(0, t); SB(); }
  xdma(2); xdma(3);
#pragma unroll
  for (int t = 0; t < 3; ++t) { wb[1][t] = wld(1, t); SB(); }
  __builtin_amdgcn_s_setprio(3);
  ST(4, 3);
  asm volatile("s_waitcnt vmcnt(8) lgkmcnt(0)\n\ts_barrier" ::: "memory");
  ST(4, 4); ST(3, 1);
  xdma(4); xdma(5);
#pragma unroll
  for (int t = 0; t < 3; ++t) { wb[2][t] = wld(2, t); SB(); }
  f4 acc[3][4];
  float r2[4];
  {
    f4 cs[3], bb[3];
#pragma unroll
    for (int t = 0; t < 3; ++t) { const AS3 f4* p = (const AS3 f4*)(smem + M_CB + ((w * 3 + t) * 4 + fg) * 32); cs[t] = p[0]; bb[t] = p[1]; }
    f4 st[4];
#pragma unroll
    for (int q = 0; q < 4; ++q) st[q] = *(const AS3 f4*)(smem + M_SS + lane * 64 + (((q + (lane >> 1)) & 3) * 16));
    float mean, var;
    ln_comb_var(st, mean, var);
    const float rstd = __builtin_amdgcn_rsqf(var), sd = var * rstd, rr = rstd * rstd;
#pragma unroll
    for (int tt = 0; tt < 4; ++tt) {
      const float nm = -__shfl(mean, tt * 16 + fi), sg = __shfl(sd, tt * 16 + fi);
      r2[tt] = __shfl(rr, tt * 16 + fi);
#pragma unroll
      for (int t = 0; t < 3; ++t) {
#pragma unroll
        for (int r = 0; r < 4; ++r) acc[t][tt][r] = sfma(cs[t][r], nm, smul(bb[t][r], sg));
        asm volatile("" : "+v"(acc[t][tt]));
      }
    }
  }
  const AS3 s8* l0 = (const AS3 s8*)smem + lane;
  const AS3 s8* l1 = opaque_lds(l0 + 4096);
  const AS3 s8* l2 = opaque_lds(l0 + 8192);
  auto lrd = [&](int piece) { return piece < 64 ? l0[piece * 64] : piece < 128 ? l1[(piece - 64) * 64] : l2[(piece - 128) * 64]; };
  const unsigned fla = lds_addr(smem) + M_FL;
  SB();
  ST(4, 5);
  s8 xf[2][4];
#pragma unroll
  for (int ph = 0; ph < 2; ++ph) {
#pragma unroll
    for (int tt = 0; tt < 4; ++tt) xf[0][tt] = lrd(ph * 72 + tt);
    SB();
    if (ph == 0) {
#pragma unroll
      for (int p = 3; p < M_PD; ++p)
#pragma unroll
        for (int t = 0; t < 3; ++t) { wb[p][t] = wld(p, t); SB(); }
      touch(1);
    }
#pragma unroll
    for (int ks = 0; ks < 18; ++ks) {
      const int u = ph * 18 + ks;
      if (M_PRIO == 2) {
        if (ks == 5) __builtin_amdgcn_s_setprio(2);
        if (ks == 10) __builtin_amdgcn_s_setprio(1);
        if (ks == 14) __builtin_amdgcn_s_setprio(0);
      }
      if (u == 5) { lds_wait_ge(fla, 12); SB(); }
#pragma unroll
      for (int t = 0; t < 3; ++t) {
#pragma unroll
        for (int tt = 0; tt < 4; ++tt) {
          if (u == 18) mfma_z(acc[t][tt], wb[u % M_PD][t], xf[ks & 1][tt]);
          else mfma_acc(acc[t][tt], wb[u % M_PD][t], xf[ks & 1][tt]);
          SB();
          if (t == 0 && ks + 1 < 18) { xf[(ks + 1) & 1][tt] = lrd(ph * 72 + (ks + 1) * 4 + tt); SB(); }
        }
        if (u == 2 && t == 0) {
          if (lane == 0) __hip_atomic_fetch_add((AS3 unsigned*)(smem + M_FL), 1u, __ATOMIC_RELAXED, __HIP_MEMORY_SCOPE_WORKGROUP);
          SB();
        }
        if (u + M_PD < 36) { wb[u % M_PD][t] = wld(u + M_PD, t); SB(); }
      }
      if (u % 6 == 4 && u / 6 + 2 < 6) touch(u / 6 + 2);
    }
    MFMA_FENCE();
    SB();
    if (ph == 0) {
      ST(3, 2);
      AS3 char* hb = (AS3 char*)smem + 73728 + ((3 * w) >> 1) * 4096 + lane * 16;
#pragma unroll
      for (int tt = 0; tt < 4; ++tt) {
        unsigned pk[3][2];
#pragma unroll
        for (int t = 0; t < 3; ++t)
#pragma unroll
          for (int r = 0; r < 2; ++r) {
            const float m0 = max2f(acc[t][tt][2 * r], 0.f), m1 = max2f(acc[t][tt][2 * r + 1], 0.f);
            pk[t][r] = cvtpk((f2){smul(m0, m0), smul(m1, m1)});
          }
        if (w & 1) {
          *(AS3 u2*)(hb + tt * 1024 + 8) = (u2){pk[0][0], pk[0][1]};
          *(AS3 u4*)(hb + tt * 1024 + 4096) = (u4){pk[1][0], pk[1][1], pk[2][0], pk[2][1]};
        } else {
          *(AS3 u4*)(hb + tt * 1024) = (u4){pk[0][0], pk[0][1], pk[1][0], pk[1][1]};
          *(AS3 u2*)(hb + tt * 1024 + 4096) = (u2){pk[2][0], pk[2][1]};
        }
      }
      SB();
      if (M_PRIO == 2) __builtin_amdgcn_s_setprio(3);
      BARRIER();
      SB();
      ST(3, 3); CY0(3);
    }
  }
  asm volatile("" :: "v"(tch));
  ST(3, 4); CY1(3);
  {
    char* pb = (char*)part + ((size_t)(slice * 128 + rt * 4) * 18 + ((3 * w) >> 1)) * 1024;
#pragma unroll
    for (int tt = 0; tt < 4; ++tt) {
      unsigned pk[3][2];
#pragma unroll
      for (int t = 0; t < 3; ++t)
#pragma unroll
        for (int r = 0; r < 2; ++r) pk[t][r] = cvtph((f2){smul(acc[t][tt][2 * r], r2[tt]), smul(acc[t][tt][2 * r + 1], r2[tt])});
      char* q = pb + tt * 18 * 1024;
      if (w & 1) {
        st_wt(q + 512 + lane * 8, (u2){pk[0][0], pk[0][1]}); st_wt(q + 1024 + lane * 16, (u4){pk[1][0], pk[1][1], pk[2][0], pk[2][1]});
      } else {
        st_wt(q + lane * 16, (u4){pk[0][0], pk[0][1], pk[1][0], pk[1][1]}); st_wt(q + 1024 + lane * 8, (u2){pk[2][0], pk[2][1]});
      }
    }
  }
  ST(3, 5);
  ST(3, 6);
  ST_OUT(3); ST_OUT(4);
}

template <bool FINAL>
__global__ __launch_bounds__(576) void kR(const unsigned* __restrict__ part, const bf16* __restrict__ b2, const bf16* __restrict__ xresF,
                                          bf16* __restrict__ xout, float* __restrict__ statsA) {
  __shared__ float red[9 * 16 * 2];
  const int tid = threadIdx.x, lane = tid & 63, w = __builtin_amdgcn_readfirstlane(tid >> 6);
  const int tt = blockIdx.x >> 1, hf = blockIdx.x & 1, ks = hf * 9 + w, unit = tt * 18 + ks;
  const int fi = lane & 15, fg = lane >> 4;
  u4 p[8];
  const char* pb = (const char*)part + (size_t)unit * 1024;
  if (w % 3 == 1) {
#pragma unroll
    for (int s = 0; s < 8; ++s) {
      const u2 a = *(const u2*)(pb + (size_t)s * 2304 * 1024 + lane * 8), b = *(const u2*)(pb + (size_t)s * 2304 * 1024 + 512 + lane * 8);
      p[s] = (u4){a[0], a[1], b[0], b[1]};
    }
  } else {
#pragma unroll
    for (int s = 0; s < 8; ++s) p[s] = *(const u4*)(pb + (size_t)s * 2304 * 1024 + lane * 16);
  }
  const u4 xr = ((const u4*)xresF)[unit * 64 + lane];
  const u4 bv = *(const u4*)(b2 + ks * 32 + fg * 8);
  u4 o;
  f2 v[4];
  f2 s2 = {0.f, 0.f};
#pragma unroll
  for (int c = 0; c < 4; ++c) {
    f2 a = ((unph(p[0][c]) + unph(p[1][c])) + (unph(p[2][c]) + unph(p[3][c]))) + ((unph(p[4][c]) + unph(p[5][c])) + (unph(p[6][c]) + unph(p[7][c])));
    a = unpk(cvtpk(a)) + unpk(bv[c]);
    a = unpk(cvtpk(a)) + unpk(xr[c]);
    o[c] = cvtpk(a);
    v[c] = unpk(o[c]);
    s2 += v[c];
  }
  if (FINAL) {
    *(u4*)(xout + (size_t)(tt * 16 + fi) * D_ + ks * 32 + fg * 8) = o;
    return;
  }
  ((u4*)xout)[unit * 64 + lane] = o;
  const float m = red_sum(s2[0] + s2[1]) * (1.0f / 32.0f);
  f2 q2 = {0.f, 0.f};
#pragma unroll
  for (int c = 0; c < 4; ++c) { const f2 d = v[c] - m; q2 += d * d; }
  const float m2 = red_sum(q2[0] + q2[1]);
  if (fg == 0) { red[(w * 16 + fi) * 2] = m; red[(w * 16 + fi) * 2 + 1] = m2; }
  __syncthreads();
  if (tid < 16) {
    float mm[9], sm = 0.f, sq = 0.f;
#pragma unroll
    for (int i = 0; i < 9; ++i) { mm[i] = red[(i * 16 + tid) * 2]; sm += mm[i]; sq += red[(i * 16 + tid) * 2 + 1]; }
    sm *= (1.0f / 9.0f);
    float dq = 0.f;
#pragma unroll
    for (int i = 0; i < 9; ++i) { const float d = mm[i] - sm; dq += d * d; }
    float2 r; r.x = sm; r.y = sq + 32.f * dq;
    *(float2*)(statsA + ((tt * 16 + tid) * 2 + hf) * 2) = r;
  }
}

__global__ __launch_bounds__(704, 3) void kB(const bf16* __restrict__ phiQ, const bf16* __restrict__ phiQt, const float* __restrict__ kvacc,
                                             const bf16* __restrict__ woF, const bf16* __restrict__ xresF, bf16* __restrict__ xout, float* __restrict__ stats) {
  __shared__ __attribute__((aligned(16))) char smem[81 * 1024 + 32 * KV_STRIDE * 2 + 24 * 1024];
  const int tid = threadIdx.x, lane = tid & 63, w = tid >> 6;
  int bid = blockIdx.x;
  bid = (bid & 7) * 32 + (bid >> 3);
  const int rt = bid >> 3, ct = bid & 7;
  ST_DECL ST(2, 0);
  if (w >= 8) {
    const int lw = w - 8;
    const char* src = (const char*)woF + (size_t)ct * 81 * 1024 + lw * 3072 + lane * 16;
    char* dst0 = smem + lw * 3072;
#pragma unroll
    for (int i = 0; i < 9; ++i) {
      const int u = i < 8 ? (i >> 1) + (i & 1) * 4 : 8;
#pragma unroll
      for (int j = 0; j < 3; ++j) glds16(src + u * 9216 + j * 1024, dst0 + u * 9216 + j * 1024);
    }
#pragma unroll
    for (int u = 0; u < 5; ++u) asm volatile("s_waitcnt vmcnt(%0)\n\ts_barrier" :: "i"(u < 4 ? 21 - 6 * u : 0) : "memory");
    return;
  }
  const int fi = lane & 15, fg = lane >> 4;
  const int tt = rt * 4 + (w & 3), hf = w >> 2;
  AS3 char* kvl = (AS3 char*)smem + 81 * 1024;
  s8 pb[16], pt[2];
  {
    f4 a[6], b[6];
#pragma unroll
    for (int i = 0; i < 6; ++i) { const f4* p = (const f4*)(kvacc + (i * 512 + tid) * 8); a[i] = p[0]; b[i] = p[1]; }
    SB();
#pragma unroll
    for (int q = 0; q < 2; ++q) pt[q] = ((const s8*)phiQt)[((size_t)tt * 4 + hf * 2 + q) * 64 + lane];
    const s8* pp = (const s8*)phiQ + ((size_t)tt * 32 + hf * 16) * 64 + lane;
#pragma unroll
    for (int h = 0; h < 16; ++h) { pb[h] = pp[h * 64]; SB(); }
#pragma unroll
    for (int i = 0; i < 6; ++i) {
      u4 o = {cvtpk((f2){a[i][0], a[i][1]}), cvtpk((f2){a[i][2], a[i][3]}), cvtpk((f2){b[i][0], b[i][1]}), cvtpk((f2){b[i][2], b[i][3]})};
      *(AS3 u4*)(kvl + (i * 512 + tid) * 16) = o;
    }
  }
  int o1[4], o2[4];
#pragma unroll
  for (int m = 0; m < 4; ++m) {
    o1[m] = fg == m ? 1024 + fi * 16 : 1520;
    o2[m] = ((fi >> 2) == m && (fi & 3) < 3) ? 1280 + (fi & 3) * 80 + fg * 16 : 1520;
  }
  const int o3 = ((fi >> 2) == fg && (fi & 3) < 3) ? fg * (KV_STRIDE * 2) + 1280 + (fi & 3) * 80 + 64 : 1520;
  const AS3 s8* wl = (const AS3 s8*)smem + lane + hf * 4 * 576;
  const AS3 char* kvh = kvl + hf * 16 * (KV_STRIDE * 2);
  f4 acc[6];
#pragma unroll
  for (int a = 0; a < 6; ++a) acc[a] = (f4){0, 0, 0, 0};
  const f4 z4 = {0, 0, 0, 0};
  unsigned treg[4];
  BARRIER();
  ST(2, 1);
#pragma unroll
  for (int u = 0; u < 4; ++u) {
    if (u > 0) BAR0();
    f4 om[4], otl = z4;
#pragma unroll
    for (int m = 0; m < 4; ++m) {
      const int h = 4 * u + m;
      const AS3 char* kb = kvh + h * (KV_STRIDE * 2);
      s8 f0 = *(const AS3 s8*)(kb + lane * 16);
      s8 f1 = *(const AS3 s8*)(kb + o1[m]);
      s8 f2_ = *(const AS3 s8*)(kb + o2[m]);
      om[m] = mfma32(f0, pb[h], z4);
      otl = mfma32(f2_, pb[h], otl);
      om[m] = mfma32(f1, pt[u >> 1], om[m]);
    }
    otl = mfma32(*(const AS3 s8*)(kvh + 4 * u * (KV_STRIDE * 2) + o3), pt[u >> 1], otl);
    const float invs = __builtin_amdgcn_rcpf(otl[2] + 1e-6f);
    treg[u] = cvtpk((f2){otl[0], otl[1]} * invs);
    float inv[4];
    agath(invs, inv);
    unsigned po[4][2];
#pragma unroll
    for (int m = 0; m < 4; ++m) { po[m][0] = cvtpk((f2){om[m][0], om[m][1]} * inv[m]); po[m][1] = cvtpk((f2){om[m][2], om[m][3]} * inv[m]); }
    s8 ob[2];
    ob[0] = mk8(po[0][0], po[0][1], po[1][0], po[1][1]);
    ob[1] = mk8(po[2][0], po[2][1], po[3][0], po[3][1]);
    s8 wt = wl[(u * 9 + 8) * 64];
#pragma unroll
    for (int s = 0; s < 2; ++s) {
#pragma unroll
      for (int nt = 0; nt < 4; ++nt) acc[nt] = mfma32(wl[(u * 9 + 4 * s + nt) * 64], ob[s], acc[nt]);
      acc[4 + s] = mfma32(wt, ob[s], acc[4 + s]);
    }
  }
  BAR0();
  {
    const AS3 s8* w8 = (const AS3 s8*)smem + lane + 8 * 576;
    const s8 ob = mk8(treg[0], treg[1], treg[2], treg[3]);
#pragma unroll
    for (int nt = 0; nt < 4; ++nt) acc[nt] = mfma32(w8[(4 * hf + nt) * 64], ob, acc[nt]);
    f4 tl = mfma32(w8[8 * 64], ob, hf ? acc[5] : acc[4]);
    if (hf) acc[5] = tl; else acc[4] = tl;
  }
  ST(2, 2);
  {
    AS3 f4* rbuf = (AS3 f4*)(smem + 81 * 1024 + 32 * KV_STRIDE * 2) + (w & 3) * 6 * 64 + lane;
    if (hf) {
#pragma unroll
      for (int a = 0; a < 6; ++a) rbuf[a * 64] = acc[a];
    }
    BARRIER();
    if (hf) return;
#pragma unroll
    for (int a = 0; a < 6; ++a) acc[a] += rbuf[a * 64];
  }
  ST(2, 3);
#pragma unroll
  for (int r = 0; r < 4; ++r) acc[4][r] += __shfl(acc[5][r], lane + 32);
  const int tok = tt * 16 + fi;
  float v[5][4];
  float sum = 0.f;
#pragma unroll
  for (int nt = 0; nt < 5; ++nt) {
    const int col = ct * 72 + nt * 16 + fg * 4;
    const bool valid = nt < 4 || fg < 2;
    const int colc = valid ? col : ct * 72;
    u2 xr = *(const u2*)(xresF + xF_index(tok, colc));
#pragma unroll
    for (int r = 0; r < 4; ++r) {
      float xv = bfu((xr[r >> 1] >> ((r & 1) * 16)) & 0xffff);
      float o = rb(xv + rb(acc[nt][r]));
      v[nt][r] = o;
      sum += valid ? o : 0.f;
    }
    if (valid) {
      u2 o2_; o2_[0] = pack2(v[nt][0], v[nt][1]); o2_[1] = pack2(v[nt][2], v[nt][3]);
      *(u2*)(xout + xF_index(tok, col)) = o2_;
    }
  }
  sum = red_sum(sum);
  const float mean = sum * (1.0f / 72.0f);
  float m2 = 0.f;
#pragma unroll
  for (int nt = 0; nt < 5; ++nt)
#pragma unroll
    for (int r = 0; r < 4; ++r) { float d = v[nt][r] - mean; m2 += (nt < 4 || fg < 2) ? d * d : 0.f; }
  m2 = red_sum(m2);
  if (fg == 0) { float2 st; st.x = mean; st.y = m2; *(float2*)(stats + (tok * 8 + ct) * 2) = st; }
  ST(2, 4);
  ST_OUT(2);
}

DI void tr88(const u4 (&r)[8], u4 (&o)[8]) {
#pragma unroll
  for (int n = 0; n < 8; ++n)
#pragma unroll
    for (int j = 0; j < 4; ++j) o[n][j] = __builtin_amdgcn_perm(r[2 * j + 1][n >> 1], r[2 * j][n >> 1], (n & 1) ? 0x07060302u : 0x05040100u);
}
DI float sum8(u4 v) {
  f2 a = unpk(v[0]) + unpk(v[1]), b = unpk(v[2]) + unpk(v[3]);
  a += b;
  return a[0] + a[1];
}
typedef unsigned u4a __attribute__((ext_vector_type(4), aligned(4)));
#define PT 576
#define PB_QKV 96
#define PB_W1 72
#define PB_W2 72
#define PB_WO 9
#define PB_TOTAL (PB_QKV + PB_W1 + PB_W2 + PB_WO)
#define PFS 65
#define WAVE_SYNC() asm volatile("s_waitcnt lgkmcnt(0)" ::: "memory")
__global__ __launch_bounds__(PT) void kP(const bf16* __restrict__ x0, const bf16* __restrict__ wqkv, const bf16* __restrict__ wphi, const bf16* __restrict__ wo,
                                          const bf16* __restrict__ w1, const bf16* __restrict__ b1, const bf16* __restrict__ w2,
                                          bf16* __restrict__ x0F, float* __restrict__ stats, bf16* __restrict__ wqkvF, float* __restrict__ csA,
                                          bf16* __restrict__ wphiF, bf16* __restrict__ woF, bf16* __restrict__ wM, float* __restrict__ cb1,
                                          float* __restrict__ kvacc) {
  __shared__ float red[72 * 18];
  __shared__ u4 stg[72 * PFS];
  int b = blockIdx.x;
  const int t = threadIdx.x, lane = t & 63, w = __builtin_amdgcn_readfirstlane(t >> 6);
  u4 r[8], o[8];
  if (b < PB_QKV) {
    if (w >= 3) {
      const int wu = b * 6 + (w - 3);
      if (wu < 256) {
        const int tt = wu >> 1, hf = wu & 1, fi = lane & 15, fg = lane >> 4;
        const bf16* src = x0 + (size_t)(tt * 16 + fi) * D_ + hf * 288 + fg * 8;
        u4 v[9];
#pragma unroll
        for (int q = 0; q < 9; ++q) v[q] = *(const u4*)(src + q * 32);
        float sm = 0.f;
#pragma unroll
        for (int q = 0; q < 9; ++q) { ((u4*)x0F)[(tt * 18 + hf * 9 + q) * 64 + lane] = v[q]; sm += sum8(v[q]); }
        const float m = red_sum(sm) * (1.0f / 288.0f);
        f2 m2 = {0.f, 0.f};
#pragma unroll
        for (int q = 0; q < 9; ++q)
#pragma unroll
          for (int c = 0; c < 4; ++c) { f2 d = unpk(v[q][c]) - m; m2 += d * d; }
        const float q2 = red_sum(m2[0] + m2[1]);
        float2 st; st.x = m; st.y = q2;
        if (fg == 0) *(float2*)(stats + ((tt * 16 + fi) * 2 + hf) * 2) = st;
      } else if (wu < 256 + 112) {
        const int idx = (wu - 256) * 64 + lane;
        const int f = (idx >> 6) % 7, hp = idx / (7 * 64);
        const int i = lane & 15, g = lane >> 4;
        unsigned short val[8];
#pragma unroll
        for (int e = 0; e < 8; ++e) {
          int bb = 0, d = 0, ft = 0; bool ok = false;
          if (f < 4) {
            bb = f; ft = i;
            if (e < 4) { d = 4 * g + e; ok = true; }
            else { const int rho = 4 * g + e - 4; d = 16 + (rho & 1); ok = (rho >> 1) == bb; }
          } else {
            const int c = i & 3, sl = i >> 2;
            bb = sl; ft = 16 + (c & 1);
            if (f == 4) { d = 4 * g + (e & 3); ok = bb == (e >> 2); }
            else if (f == 5) { d = 4 * g + (e & 3); ok = bb == 2 + (e >> 2); }
            else { const int rho = 4 * g + e; d = 16 + (rho & 1); ok = e < 4 && (rho >> 1) == bb; }
            ok = ok && c < 2;
          }
          unsigned short wv = __builtin_bit_cast(unsigned short, wphi[((2 * hp + (bb & 1)) * DH_ + d) * DH_ + ft]);
          val[e] = ok ? wv : (unsigned short)0;
        }
        u4 ov;
#pragma unroll
        for (int e = 0; e < 4; ++e) ov[e] = (unsigned)val[2 * e] | ((unsigned)val[2 * e + 1] << 16);
        ((u4*)wphiF)[idx] = ov;
      } else if (wu < 256 + 112 + 192) {
        ((f4*)kvacc)[(wu - 368) * 64 + lane] = (f4){0.f, 0.f, 0.f, 0.f};
      }
      return;
    }
    const int hp = b / 6, j = b % 6, half = t & 1, kb = t >> 1;
    u4* fdst = (u4*)wqkvF + (size_t)hp * 126 * 64;
    if (t < 144) {
      const int jp = j < 4 ? j ^ (2 * (hp & 1)) : j;
      const bf16* src = wqkv + (size_t)kb * 8 * (3 * D_) + 576 * (jp >> 1) + 18 * (2 * hp + (jp & 1)) + 8 * half;
      unsigned tl[8];
#pragma unroll
      for (int e = 0; e < 8; ++e) r[e] = *(const u4a*)(src + e * (3 * D_));
#pragma unroll
      for (int e = 0; e < 8; ++e) tl[e] = *(const unsigned*)(src + e * (3 * D_) + (half ? 8 : 16));
      tr88(r, o);
      u4* dst = stg + (kb >> 2) * PFS + (kb & 3) * 16 + half * 8;
#pragma unroll
      for (int n = 0; n < 8; ++n) { dst[n] = o[n]; red[kb * 18 + half * 8 + n] = sum8(o[n]); }
      u4 tv;
#pragma unroll
      for (int q = 0; q < 4; ++q) tv[q] = __builtin_amdgcn_perm(tl[2 * q + 1], tl[2 * q], half ? 0x07060302u : 0x05040100u);
      u4* td = fdst + ((kb >> 2) * 7 + 6) * 64 + (kb & 3) * 16;
      td[2 * j + half] = tv;
      if (j >= 4) td[2 * j + 4 + half] = (u4){0u, 0u, 0u, 0u};
      red[kb * 18 + 16 + half] = sum8(tv);
    }
    __syncthreads();
#pragma unroll
    for (int f = 0; f < 6; ++f) { const int ks = w * 6 + f; fdst[(ks * 7 + j) * 64 + lane] = stg[ks * PFS + lane]; }
    if (t < 144) {
      const int c = t >> 3, p = t & 7;
      float sm = 0.f;
#pragma unroll
      for (int k = 0; k < 9; ++k) sm += red[(p * 9 + k) * 18 + c];
      sm += __shfl_xor(sm, 1); sm += __shfl_xor(sm, 2); sm += __shfl_xor(sm, 4);
      if (p == 0) csA[hp * 112 + (c < 16 ? j * 16 + c : 96 + 2 * j + c - 16)] = sm;
      if (j >= 4 && t < 2) csA[hp * 112 + 96 + 2 * j + 4 + t] = 0.f;
    }
    return;
  }
  b -= PB_QKV;
  u4* wstg = stg + w * 8 * PFS;
  if (b < PB_W1) {
    const int oc = lane & 7, kb = lane >> 3;
    const bf16* src = w1 + (size_t)(w * 64 + kb * 8) * F_ + b * 64 + oc * 8;
#pragma unroll
    for (int e = 0; e < 8; ++e) r[e] = *(const u4*)(src + e * F_);
    tr88(r, o);
    u4* dst = wstg + ((kb >> 2) * 4 + (oc >> 1)) * PFS + (kb & 3) * 16 + (oc & 1) * 8;
    float sm[8];
#pragma unroll
    for (int n = 0; n < 8; ++n) { dst[n] = o[n]; sm[n] = sum8(o[n]); }
    WAVE_SYNC();
#pragma unroll
    for (int f = 0; f < 8; ++f) {
      const int T = b * 4 + (f & 3);
      ((u4*)wM)[((size_t)(T / 3) * 108 + (w * 2 + (f >> 2)) * 3 + T % 3) * 64 + lane] = wstg[f * PFS + lane];
    }
    const bool b0 = kb & 1, b1_ = kb & 2, b2 = kb & 4;
    float v[4], u[2];
#pragma unroll
    for (int j = 0; j < 4; ++j) v[j] = (b0 ? sm[4 + j] : sm[j]) + __shfl_xor(b0 ? sm[j] : sm[4 + j], 8);
#pragma unroll
    for (int j = 0; j < 2; ++j) u[j] = (b1_ ? v[2 + j] : v[j]) + __shfl_xor(b1_ ? v[j] : v[2 + j], 16);
    red[w * 64 + oc * 8 + (b0 ? 4 : 0) + (b1_ ? 2 : 0) + (b2 ? 1 : 0)] = (b2 ? u[1] : u[0]) + __shfl_xor(b2 ? u[0] : u[1], 32);
    __syncthreads();
    if (t < 64) {
      float s0 = 0.f;
#pragma unroll
      for (int k = 0; k < 9; ++k) s0 += red[k * 64 + t];
      float* q = cb1 + (b * 16 + (t >> 2)) * 8 + (t & 3);
      q[0] = s0;
      q[4] = b2f(b1[b * 64 + t]);
    }
    return;
  }
  b -= PB_W1;
  if (b < PB_W2) {
    const int oc = lane & 7, kbl = lane >> 3, g = kbl & 3;
    const bf16* src = w2 + (size_t)(64 * b + 32 * (kbl >> 2) + 4 * g) * D_ + w * 64 + oc * 8;
#pragma unroll
    for (int e = 0; e < 8; ++e) r[e] = *(const u4*)(src + (e < 4 ? e : 12 + e) * D_);
    tr88(r, o);
    u4* dst = wstg + ((kbl >> 2) * 4 + (oc >> 2) * 2) * PFS + g * 16 + (oc & 3) * 4;
#pragma unroll
    for (int n = 0; n < 8; ++n) dst[(n >> 2) * PFS + (n & 3)] = o[n];
    WAVE_SYNC();
#pragma unroll
    for (int f = 0; f < 8; ++f) {
      const int T = w * 4 + (f & 3), ksg = b * 2 + (f >> 2);
      ((u4*)wM)[((size_t)((ksg / 18) * 12 + T / 3) * 108 + 54 + (ksg % 18) * 3 + T % 3) * 64 + lane] = wstg[f * PFS + lane];
    }
    return;
  }
  b -= PB_W2;
  {
    const int oc = t % 72, kbl = t / 72, ks = b * 2 + (kbl >> 2), g = kbl & 3;
    const bf16* src = wo + oc * 8;
#pragma unroll
    for (int e = 0; e < 8; ++e) r[e] = *(const u4*)(src + (size_t)wo_k(ks, g, e) * D_);
    tr88(r, o);
    const int q = oc % 9;
    const int u = q < 8 ? (ks & 1) * 4 + (q >> 1) : 8, i0 = q < 8 ? (q & 1) * 8 : (ks & 1) * 8;
    u4* dst = stg + ((oc / 9) * 9 + u) * PFS + g * 16 + i0;
#pragma unroll
    for (int n = 0; n < 8; ++n) dst[n] = o[n];
    __syncthreads();
#pragma unroll
    for (int f = 0; f < 8; ++f) {
      const int fr = w * 8 + f;
      ((u4*)woF)[(((size_t)(fr / 9) * 9 + b) * 9 + fr % 9) * 64 + lane] = stg[fr * PFS + lane];
    }
  }
}

extern "C" void kernel_launch(void* const* d_in, const int* in_sizes, int n_in,
                              void* d_out, int out_size, void* d_ws, size_t ws_size,
                              hipStream_t stream) {
  const bf16* x0 = (const bf16*)d_in[0];
  const bf16* wqkv = (const bf16*)d_in[1];
  const bf16* wphi = (const bf16*)d_in[2];
  const bf16* wo = (const bf16*)d_in[3];
  const bf16* w1 = (const bf16*)d_in[5];
  const bf16* b1 = (const bf16*)d_in[6];
  const bf16* w2 = (const bf16*)d_in[7];
  const bf16* b2 = (const bf16*)d_in[8];
  char* ws = (char*)d_ws;
  size_t off = 0;
  auto alloc = [&](size_t bytes) { void* p = ws + off; off += (bytes + 255) & ~(size_t)255; return p; };
  bf16* x0F = (bf16*)alloc(S_ * D_ * 2);
  bf16* x1F = (bf16*)alloc(S_ * D_ * 2);
  bf16* x2F = (bf16*)alloc(S_ * D_ * 2);
  unsigned* part = (unsigned*)alloc((size_t)8 * S_ * D_ * 2);
  bf16* wM = (bf16*)alloc((size_t)2 * D_ * F_ * 2);
  bf16* wqkvF = (bf16*)alloc((size_t)16 * 126 * 1024 + 2048);
  bf16* woF = (bf16*)alloc((size_t)8 * 81 * 1024);
  bf16* wphiF = (bf16*)alloc((size_t)16 * 7 * 1024);
  bf16* phiQ = (bf16*)alloc((size_t)128 * 32 * 1024);
  bf16* phiQt = (bf16*)alloc((size_t)128 * 8 * 512);
  float* kvacc = (float*)alloc(2 * 32 * KV_STRIDE * 4);
  float* cb1 = (float*)alloc(F_ * 8 + 1024);
  float* csA = (float*)alloc(3 * 16 * 112 * 4);
  float* stats1 = (float*)alloc(S_ * 16 * 4 + 65536);
  float* stats = (float*)alloc(S_ * 16 * 4 + 65536);
  kP<<<PB_TOTAL, PT, 0, stream>>>(x0, wqkv, wphi, wo, w1, b1, w2, x0F, stats, wqkvF, csA, wphiF, woF, wM, cb1, kvacc);
  const bf16* xin = x0F;
  for (int layer = 0; layer < 2; ++layer) {
    float* kv = kvacc + layer * 32 * KV_STRIDE;
    kA<<<256, 512, 0, stream>>>(xin, wqkvF, csA, wphiF, stats, phiQ, phiQt, kv);
    kB<<<256, 704, 0, stream>>>(phiQ, phiQt, kv, woF, xin, x1F, stats1);
    kM<<<256, 768, 0, stream>>>(x1F, wM, cb1, stats1, part);
    if (layer == 0) kR<false><<<256, 576, 0, stream>>>(part, b2, x1F, x2F, stats);
    else kR<true><<<256, 576, 0, stream>>>(part, b2, x1F, (bf16*)d_out, stats);
    xin = x2F;
  }
}
```

```cpp
#include <hip/hip_runtime.h>
#include <hip/hip_bf16.h>
#include <stdint.h>

typedef __hip_bfloat16 bf16;
typedef short s4 __attribute__((ext_vector_type(4)));
typedef short s8 __attribute__((ext_vector_type(8)));
typedef float f4 __attribute__((ext_vector_type(4)));
typedef __bf16 bf8 __attribute__((ext_vector_type(8)));
typedef unsigned u4 __attribute__((ext_vector_type(4)));
typedef unsigned u2 __attribute__((ext_vector_type(2)));
#define AS3 __attribute__((address_space(3)))
#define AS1 __attribute__((address_space(1)))
#define DI __device__ __forceinline__

#define S_ 2048
#define D_ 576
#define H_ 32
#define DH_ 18
#define F_ 4608

DI float b2f(bf16 x) { return __bfloat162float(x); }
DI bf16 f2b(float x) { return __float2bfloat16(x); }
DI float rb(float x) { return b2f(f2b(x)); }
DI float bfu(unsigned short u) { return __uint_as_float(((unsigned)u) << 16); }
DI unsigned pack2(float a, float b) {
  bf16 x = f2b(a), y = f2b(b);
  return (unsigned)__builtin_bit_cast(unsigned short, x) | ((unsigned)__builtin_bit_cast(unsigned short, y) << 16);
}
typedef float f2 __attribute__((ext_vector_type(2)));
typedef short s2 __attribute__((ext_vector_type(2)));
typedef __bf16 bf2 __attribute__((ext_vector_type(2)));
DI unsigned cvtpk(f2 v) { return __builtin_bit_cast(unsigned, __builtin_convertvector(v, bf2)); }
DI f2 unpk(unsigned u) { f2 r; r[0] = __uint_as_float(u << 16); r[1] = __uint_as_float(u & 0xffff0000u); return r; }
DI f4 mfma32(s8 a, s8 b, f4 c) { return __builtin_amdgcn_mfma_f32_16x16x32_bf16(__builtin_bit_cast(bf8, a), __builtin_bit_cast(bf8, b), c, 0, 0, 0); }
DI void mfma_acc(f4& c, s8 a, s8 b) { asm volatile("v_mfma_f32_16x16x32_bf16 %0, %1, %2, %0" : "+v"(c) : "v"(a), "v"(b)); }
#define MFMA_FENCE() asm volatile("s_nop 7\n\ts_nop 3" ::: "memory")
#define LDS_R128(d, addr, off) asm volatile("ds_read_b128 %0, %1 offset:%2" : "=v"(d) : "v"(addr), "i"(off))
#define LDS_RTR(d, addr, off) asm volatile("ds_read_b64_tr_b16 %0, %1 offset:%2" : "=v"(d) : "v"(addr), "i"(off))
DI unsigned lds_addr(const void* p) { return (unsigned)(uintptr_t)(AS3 const char*)p; }
DI const AS3 s8* opaque_lds(const AS3 s8* p) { unsigned a = (unsigned)(uintptr_t)p; asm volatile("" : "+v"(a)); return (const AS3 s8*)(uintptr_t)a; }
DI void glds16(const void* g, char* l) { __builtin_amdgcn_global_load_lds((const AS1 unsigned*)g, (AS3 unsigned*)l, 16, 0, 0); }
DI void glds16a(const void* g, unsigned l) { asm volatile("s_mov_b32 m0, %1\n\ts_nop 0\n\tglobal_load_lds_dwordx4 %0, off" :: "v"(g), "s"(l) : "memory"); }
#define WAIT_V(n) asm volatile("s_waitcnt vmcnt(" #n ")" ::: "memory")

#define ST_DECL
#define ST(K, i)
#define CY0(K)
#define CY1(K)
#define ST_OUT(K)
#define ST_OUTP(K)
DI size_t hF_index(int t, int f) {
  int ks = f >> 5, r = f & 31, hi = r >> 4, g = (r & 15) >> 2, e = (r & 3) | (hi << 2);
  return ((((size_t)(t >> 4) * (F_ / 32) + ks) * 64 + g * 16 + (t & 15)) << 3) + e;
}
DI size_t xF_index(int t, int k) { return ((((size_t)(t >> 4) * 18 + (k >> 5)) * 64 + ((k >> 3) & 3) * 16 + (t & 15)) << 3) + (k & 7); }
#define SB() __builtin_amdgcn_sched_barrier(0)
#define BAR0() asm volatile("s_barrier" ::: "memory")
#define BARRIER() asm volatile("s_waitcnt lgkmcnt(0)\n\ts_barrier" ::: "memory")

DI void ln_comb(const f4 (&p)[4], float& mean, float& rstd);
DI void ln_stats(const float* __restrict__ stats, int tok, float& mean, float& rstd) {
  const f4* p = (const f4*)(stats + tok * 16);
  f4 q[4] = {p[0], p[1], p[2], p[3]};
  ln_comb(q, mean, rstd);
}
DI void ln_comb(const f4 (&p)[4], float& mean, float& rstd) {
  f4 a = p[0], b = p[1], c = p[2], d = p[3];
  float m = (a[0] + a[2] + b[0] + b[2] + c[0] + c[2] + d[0] + d[2]) * 0.125f;
  float m2 = a[1] + a[3] + b[1] + b[3] + c[1] + c[3] + d[1] + d[3];
  float q = 0.f, t;
  t = a[0] - m; q += t * t; t = a[2] - m; q += t * t; t = b[0] - m; q += t * t; t = b[2] - m; q += t * t;
  t = c[0] - m; q += t * t; t = c[2] - m; q += t * t; t = d[0] - m; q += t * t; t = d[2] - m; q += t * t;
  float var = (m2 + 72.f * q) * (1.0f / 576.0f);
  mean = m; rstd = 1.0f / sqrtf(var + 1e-5f);
}

DI void ln_comb_var(const f4 (&p)[4], float& mean, float& var) {
  f4 a = p[0], b = p[1], c = p[2], d = p[3];
  float m = ((a[0] + a[2]) + (b[0] + b[2]) + ((c[0] + c[2]) + (d[0] + d[2]))) * 0.125f;
  float m2 = (a[1] + a[3]) + (b[1] + b[3]) + ((c[1] + c[3]) + (d[1] + d[3]));
  float q = 0.f, t;
  t = a[0] - m; q += t * t; t = a[2] - m; q += t * t; t = b[0] - m; q += t * t; t = b[2] - m; q += t * t;
  t = c[0] - m; q += t * t; t = c[2] - m; q += t * t; t = d[0] - m; q += t * t; t = d[2] - m; q += t * t;
  mean = m; var = (m2 + 72.f * q) * (1.0f / 576.0f) + 1e-5f;
}
DI void ln_comb2(f4 a, float& mean, float& rstd) {
  const float m = (a[0] + a[2]) * 0.5f, dlt = a[0] - a[2];
  const float var = (a[1] + a[3] + 144.f * dlt * dlt) * (1.0f / 576.0f);
  mean = m; rstd = 1.0f / sqrtf(var + 1e-5f);
}
DI int qkv_col(int hp, int j, int i) {
  if (j < 6) return 576 * (j >> 1) + 18 * (2 * hp + (j & 1)) + i;
  if (i >= 12) return -1;
  int t = i >> 1;
  return 576 * (t >> 1) + 18 * (2 * hp + (t & 1)) + 16 + (i & 1);
}
DI int wo_k(int s, int g, int e) {
  if (s < 16) return 18 * (2 * s + (e >> 2)) + 4 * g + (e & 3);
  int q = 4 * (s - 16) + (e >> 1);
  return 18 * (4 * q + g) + 16 + (e & 1);
}
DI float red_max(float x) {
  auto a = __builtin_amdgcn_permlane16_swap(__float_as_uint(x), __float_as_uint(x), false, false);
  x = fmaxf(__uint_as_float(a[0]), __uint_as_float(a[1]));
  auto b = __builtin_amdgcn_permlane32_swap(__float_as_uint(x), __float_as_uint(x), false, false);
  return fmaxf(__uint_as_float(b[0]), __uint_as_float(b[1]));
}
DI float red_min(float x) {
  auto a = __builtin_amdgcn_permlane16_swap(__float_as_uint(x), __float_as_uint(x), false, false);
  x = fminf(__uint_as_float(a[0]), __uint_as_float(a[1]));
  auto b = __builtin_amdgcn_permlane32_swap(__float_as_uint(x), __float_as_uint(x), false, false);
  return fminf(__uint_as_float(b[0]), __uint_as_float(b[1]));
}
DI float red_sum(float x) {
  auto a = __builtin_amdgcn_permlane16_swap(__float_as_uint(x), __float_as_uint(x), false, false);
  x = __uint_as_float(a[0]) + __uint_as_float(a[1]);
  auto b = __builtin_amdgcn_permlane32_swap(__float_as_uint(x), __float_as_uint(x), false, false);
  return __uint_as_float(b[0]) + __uint_as_float(b[1]);
}
DI s8 mk8(unsigned a, unsigned b, unsigned c, unsigned d) { u4 v = {a, b, c, d}; return __builtin_bit_cast(s8, v); }
DI s8 zx8(s4 a) { return __builtin_shufflevector(a, (s4){0, 0, 0, 0}, 0, 1, 2, 3, 4, 5, 6, 7); }
DI s4 trread(const AS3 char* p) { return __builtin_amdgcn_ds_read_tr16_b64_v4i16((AS3 s4*)p); }
#define KV_STRIDE 768

DI float max2f(float a, float b) { return __builtin_elementwise_maximum(a, b); }
DI float min2f(float a, float b) { return __builtin_elementwise_minimum(a, b); }
DI float max3f(float a, float b, float c) { return max2f(max2f(a, b), c); }
DI float min3f(float a, float b, float c) { return min2f(min2f(a, b), c); }
DI void swap16(float& a, float& b) { auto r = __builtin_amdgcn_permlane16_swap(__float_as_uint(a), __float_as_uint(b), false, false); a = __uint_as_float(r[0]); b = __uint_as_float(r[1]); }
DI void swap32(float& a, float& b) { auto r = __builtin_amdgcn_permlane32_swap(__float_as_uint(a), __float_as_uint(b), false, false); a = __uint_as_float(r[0]); b = __uint_as_float(r[1]); }
template <int OP> DI float op2(float a, float b) { return OP == 0 ? max2f(a, b) : OP == 1 ? min2f(a, b) : a + b; }
template <int OP> DI float rscat(float a, float b, float c, float d) {
  swap16(a, b); swap16(c, d);
  float x = op2<OP>(a, b), y = op2<OP>(c, d);
  swap32(x, y);
  return op2<OP>(x, y);
}
DI void agath(float r, float (&o)[4]) {
  float x = r, y = r;
  swap16(x, y);
  float x2 = x, y2 = y;
  swap32(x, x2); swap32(y, y2);
  o[0] = x; o[2] = x2; o[1] = y; o[3] = y2;
}
#define A_XW 6
#define A_GD 3

DI void lds_wait_ge(unsigned addr, unsigned n) {
  unsigned v, sv;
  asm volatile("LW%=:\n\tds_read_b32 %0, %2\n\ts_waitcnt lgkmcnt(0)\n\tv_readfirstlane_b32 %1, %0\n\ts_cmp_ge_u32 %1, %3\n\ts_cbranch_scc1 LD%=\n\ts_sleep 1\n\ts_branch LW%=\nLD%=:"
               : "=&v"(v), "=&s"(sv) : "v"(addr), "i"(n) : "memory", "scc");
}
DI void st_wt(void* p, u4 v) { asm volatile("global_store_dwordx4 %0, %1, off sc1\n\ts_nop 1" :: "v"(p), "v"(v) : "memory"); }
DI void st_wt(void* p, u2 v) { asm volatile("global_store_dwordx2 %0, %1, off sc1\n\ts_nop 1" :: "v"(p), "v"(v) : "memory"); }
DI void tr88(const u4 (&r)[8], u4 (&o)[8]) {
#pragma unroll
  for (int n = 0; n < 8; ++n)
#pragma unroll
    for (int j = 0; j < 4; ++j) o[n][j] = __builtin_amdgcn_perm(r[2 * j + 1][n >> 1], r[2 * j][n >> 1], (n & 1) ? 0x07060302u : 0x05040100u);
}
DI float sum8(u4 v) {
  f2 a = unpk(v[0]) + unpk(v[1]), b = unpk(v[2]) + unpk(v[3]);
  a += b;
  return a[0] + a[1];
}
#define PFS 65
#define WAVE_SYNC() asm volatile("s_waitcnt lgkmcnt(0)" ::: "memory")
#define PU_WO 648
#define PU_W2 729
#define PU_END 1377
#define PREP_PARAMS const bf16* p_w1, const bf16* p_w2, const bf16* p_wo, bf16* p_wM, bf16* p_woF, float* p_cb1
struct PrepArgs { const bf16* w1; const bf16* w2; const bf16* wo; bf16* wM; bf16* woF; float* cb1; };
DI u4 ldnt(const void* p) { return __builtin_nontemporal_load((const u4*)p); }
DI void prep_load(const PrepArgs& a, int u, int lane, u4 (&r)[8]) {
  const int oc = lane & 7, kb = lane >> 3, g = kb & 3;
  if (u < PU_WO) {
    const bf16* src = a.w1 + (size_t)((u % 9) * 64 + kb * 8) * F_ + (u / 9) * 64 + oc * 8;
#pragma unroll
    for (int e = 0; e < 8; ++e) r[e] = ldnt(src + e * F_);
  } else if (u < PU_W2) {
    const int v = u - PU_WO;
    const bf16* src = a.wo + (v % 9) * 64 + oc * 8;
#pragma unroll
    for (int e = 0; e < 8; ++e) r[e] = ldnt(src + (size_t)wo_k((v / 9) * 2 + (kb >> 2), g, e) * D_);
  } else {
    const int v = u - PU_W2;
    const bf16* src = a.w2 + (size_t)(64 * (v / 9) + 32 * (kb >> 2) + 4 * g) * D_ + (v % 9) * 64 + oc * 8;
#pragma unroll
    for (int e = 0; e < 8; ++e) r[e] = ldnt(src + (e < 4 ? e : 12 + e) * D_);
  }
}
DI void prep_finish(const PrepArgs& a, int u, int lane, const u4 (&r)[8], AS3 u4* wstg) {
  const int oc = lane & 7, kb = lane >> 3, g = kb & 3;
  u4 o[8];
  tr88(r, o);
  if (u < PU_W2) {
    AS3 u4* dst = wstg + ((kb >> 2) * 4 + (oc >> 1)) * PFS + g * 16 + (oc & 1) * 8;
#pragma unroll
    for (int n = 0; n < 8; ++n) dst[n] = o[n];
  } else {
    AS3 u4* dst = wstg + ((kb >> 2) * 4 + (oc >> 2) * 2) * PFS + g * 16 + (oc & 3) * 4;
#pragma unroll
    for (int n = 0; n < 8; ++n) dst[(n >> 2) * PFS + (n & 3)] = o[n];
  }
  WAVE_SYNC();
#pragma unroll
  for (int f = 0; f < 8; ++f) o[f] = wstg[f * PFS + lane];
  asm volatile("" : "+v"(o[0]), "+v"(o[1]), "+v"(o[2]), "+v"(o[3]), "+v"(o[4]), "+v"(o[5]), "+v"(o[6]), "+v"(o[7]));
  if (u < PU_WO) {
    const int b = u / 9, w = u % 9;
#pragma unroll
    for (int f = 0; f < 8; ++f) {
      const int T = b * 4 + (f & 3);
      st_wt((u4*)a.wM + ((size_t)(T / 3) * 108 + (w * 2 + (f >> 2)) * 3 + T % 3) * 64 + lane, o[f]);
    }
    const float cs = rscat<2>(sum8(o[0]) + sum8(o[4]), sum8(o[1]) + sum8(o[5]), sum8(o[2]) + sum8(o[6]), sum8(o[3]) + sum8(o[7]));
    unsafeAtomicAdd(a.cb1 + (b * 16 + (lane >> 2)) * 8 + (lane & 3), cs);
  } else if (u < PU_W2) {
    const int v = u - PU_WO, b = v / 9, w = v % 9;
#pragma unroll
    for (int f = 0; f < 8; ++f) st_wt((u4*)a.woF + ((size_t)(w * 4 + (f & 3)) * 18 + b * 2 + (f >> 2)) * 64 + lane, o[f]);
  } else {
    const int v = u - PU_W2, b = v / 9, w = v % 9;
#pragma unroll
    for (int f = 0; f < 8; ++f) {
      const int T = w * 4 + (f & 3), ksg = b * 2 + (f >> 2);
      st_wt((u4*)a.wM + ((size_t)((ksg / 18) * 12 + T / 3) * 108 + 54 + (ksg % 18) * 3 + T % 3) * 64 + lane, o[f]);
    }
  }
}
DI void phi4(const s8 (&wp)[5], const u4 (&m)[2], const u2 (&t)[2], u4 (&om)[4], u2& ot) {
  const f4 z4 = {0, 0, 0, 0};
  const float L2E = 1.4426950408889634f;
  f4 P[4], Pt;
#pragma unroll
  for (int b = 0; b < 4; ++b) P[b] = mfma32(wp[b & 1], mk8(m[b >> 1][2 * (b & 1)], m[b >> 1][2 * (b & 1) + 1], t[b >> 1][0], t[b >> 1][1]), z4);
  Pt = mfma32(wp[2], __builtin_bit_cast(s8, m[0]), z4);
  Pt = mfma32(wp[3], __builtin_bit_cast(s8, m[1]), Pt);
  Pt = mfma32(wp[4], mk8(t[0][0], t[0][1], t[1][0], t[1][1]), Pt);
  float hi[4], lo[4];
#pragma unroll
  for (int b = 0; b < 4; ++b) { hi[b] = max2f(max3f(P[b][0], P[b][1], P[b][2]), P[b][3]); lo[b] = min2f(min3f(P[b][0], P[b][1], P[b][2]), P[b][3]); }
  const float mxs = max3f(rscat<0>(hi[0], hi[1], hi[2], hi[3]), Pt[0], Pt[1]) * -L2E;
  const float mns = min3f(rscat<1>(lo[0], lo[1], lo[2], lo[3]), Pt[0], Pt[1]) * L2E;
  float mx[4], mn[4];
  agath(mxs, mx); agath(mns, mn);
  float ep[4][4], en[4][4], sp[4], sn[4];
#pragma unroll
  for (int b = 0; b < 4; ++b) {
#pragma unroll
    for (int r = 0; r < 4; ++r) { ep[b][r] = __builtin_amdgcn_exp2f(__builtin_fmaf(P[b][r], L2E, mx[b])); en[b][r] = __builtin_amdgcn_exp2f(__builtin_fmaf(P[b][r], -L2E, mn[b])); }
    const f2 s1 = (f2){ep[b][0], ep[b][1]} + (f2){ep[b][2], ep[b][3]}, s2 = (f2){en[b][0], en[b][1]} + (f2){en[b][2], en[b][3]};
    sp[b] = s1[0] + s1[1]; sn[b] = s2[0] + s2[1];
  }
  float tp[2], tn[2];
#pragma unroll
  for (int c = 0; c < 2; ++c) { tp[c] = __builtin_amdgcn_exp2f(__builtin_fmaf(Pt[c], L2E, mxs)); tn[c] = __builtin_amdgcn_exp2f(__builtin_fmaf(Pt[c], -L2E, mns)); }
  const float ips = __builtin_amdgcn_rcpf(rscat<2>(sp[0], sp[1], sp[2], sp[3]) + (tp[0] + tp[1]));
  const float ins = __builtin_amdgcn_rcpf(rscat<2>(sn[0], sn[1], sn[2], sn[3]) + (tn[0] + tn[1]));
  float ip[4], in[4];
  agath(ips, ip); agath(ins, in);
#pragma unroll
  for (int b = 0; b < 4; ++b) {
    om[b][0] = cvtpk((f2){ep[b][0], ep[b][1]} * ip[b]); om[b][1] = cvtpk((f2){ep[b][2], ep[b][3]} * ip[b]);
    om[b][2] = cvtpk((f2){en[b][0], en[b][1]} * in[b]); om[b][3] = cvtpk((f2){en[b][2], en[b][3]} * in[b]);
  }
  ot = (u2){cvtpk((f2){tp[0], tp[1]} * ips), cvtpk((f2){tn[0], tn[1]} * ins)};
}
#define A_PRIO 2
#define A_FL (128 * 1024)
#define A_HO (64 * 1024)
#define KVQ 3072
#define A_PG 8
#define A_PP 3
#define A_PU 2
template <bool PREP>
__global__ __launch_bounds__(PREP ? 704 : 512, PREP ? 3 : 2) void kA(const bf16* __restrict__ xF, const bf16* __restrict__ wqkvF, const float* __restrict__ csA,
                                             const bf16* __restrict__ wphiF, const float* __restrict__ stats,
                                             bf16* __restrict__ phiQ, bf16* __restrict__ phiQt, float* __restrict__ kvacc, PREP_PARAMS) {
  __shared__ __attribute__((aligned(16))) char smem[128 * 1024 + 64];
  const PrepArgs pa = {p_w1, p_w2, p_wo, p_wM, p_woF, p_cb1};
  const int tid = threadIdx.x, lane = tid & 63, w = __builtin_amdgcn_readfirstlane(tid >> 6);
  const int xcd = blockIdx.x & 7, li = blockIdx.x >> 3;
  const int rt = (xcd & 3) * 4 + (li & 3), hp = (xcd >> 2) * 8 + (li >> 2);
  const int fi = lane & 15, fg = lane >> 4;
  const int tt0 = rt * 8 + (w & 3) * 2;
  ST_DECL ST(1, 0);
  if (PREP && w >= 8) {
    const int u = blockIdx.x * 3 + (w - 8), u2_ = u + 768;
    const bool two = A_PU > 1 && u2_ < PU_END;
    u4 r[8], r2[8];
    __builtin_amdgcn_s_setprio(A_PP);
#pragma unroll
    for (int g = 0; g < 9; ++g) { BAR0(); if (g == A_PG) { prep_load(pa, u, lane, r); if (two) prep_load(pa, u2_, lane, r2); SB(); } }
    ST(1, 2);
    asm volatile("" :: "v"(r[7]));
    ST(1, 3);
    if (two) asm volatile("" :: "v"(r2[7]));
    prep_finish(pa, u, lane, r, (AS3 u4*)(smem + 76 * 1024) + (w - 8) * 8 * PFS);
    if (two) prep_finish(pa, u2_, lane, r2, (AS3 u4*)(smem + 76 * 1024) + (w - 8) * 8 * PFS);
    ST(1, 5);
    ST(1, 6);
    ST_OUTP(1);
    return;
  }
  if (w >= 4) {
    const int lw = w - 4;
    const char* src = (const char*)wqkvF + (size_t)hp * 126 * 1024 + lw * 1024 + lane * 16;
    const unsigned dst = lds_addr(smem) + lw * 1024;
    auto cum = [](int g) { return g >= 8 ? 32 : (14 * (g + 1) + 3) / 4; };
#pragma unroll
    for (int i = 0; i < cum(A_GD - 1); ++i) glds16a(src + i * 4096, dst + i * 4096);
    if (lane < 2) *(AS3 unsigned*)(smem + A_FL + lw * 4 + lane * 16) = 0u;
    s8 wp[5];
    const s8* wsrc = (const s8*)wphiF + hp * 8 * 64 + lane;
#pragma unroll
    for (int g = 0; g < 9; ++g) {
      asm volatile("s_waitcnt vmcnt(%0) lgkmcnt(0)\n\ts_barrier" :: "i"(cum(g + A_GD - 1) - cum(g) + (g > 8 - A_GD ? 5 : 0)) : "memory");
#pragma unroll
      for (int i = cum(g + A_GD - 1); i < cum(g + A_GD); ++i) glds16a(src + i * 4096, dst + i * 4096);
      if (g == 8 - A_GD) {
#pragma unroll
        for (int f = 0; f < 5; ++f) asm volatile("global_load_dwordx4 %0, %1, off" : "=v"(wp[f]) : "v"(wsrc + (f < 2 ? f : f + 2) * 64) : "memory");
      }
    }
    asm volatile("s_waitcnt vmcnt(0)" : "+v"(wp[0]), "+v"(wp[1]), "+v"(wp[2]), "+v"(wp[3]), "+v"(wp[4]) :: "memory");
    lds_wait_ge(lds_addr(smem) + A_FL + lw * 4, 1);
    ST(1, 2);
    const AS3 u4* ho = (const AS3 u4*)(smem + A_HO + lw * 3072) + lane;
    const u4 m[2] = {ho[0], ho[64]};
    const u4 tq = ho[128];
    const u2 t[2] = {(u2){tq[0], tq[1]}, (u2){tq[2], tq[3]}};
    u4 om[4]; u2 ot;
    phi4(wp, m, t, om, ot);
#pragma unroll
    for (int b = 0; b < 4; ++b) st_wt((u4*)phiQ + ((size_t)(tt0 + (b >> 1)) * 32 + 2 * hp + (b & 1)) * 64 + lane, om[b]);
    st_wt((char*)phiQt + ((((size_t)(tt0 >> 1) * 8 + (hp >> 1)) * 64 + ((hp & 1) * 2 + (fg & 1)) * 16 + fi) * 16 + (fg >> 1) * 8), ot);
    ST(1, 3);
    ST_OUTP(1);
    return;
  }
  const s8* xp = (const s8*)xF + (size_t)tt0 * 18 * 64 + lane;
  const AS3 s8* wl = (const AS3 s8*)smem + lane;
  const AS3 s8* wl2 = opaque_lds(wl + 4096);
  auto wrd = [&](int piece) { return piece < 64 ? wl[piece * 64] : wl2[(piece - 64) * 64]; };
  float mean[2], rstd[2];
  f4 acc[7][2];
  s8 xb[18][2];
  {
    f4 st[2], cs[7];
#pragma unroll
    for (int mt = 0; mt < 2; ++mt) st[mt] = *(const f4*)(stats + ((tt0 + mt) * 16 + fi) * 4);
#pragma unroll
    for (int j = 0; j < 7; ++j) cs[j] = *(const f4*)(csA + hp * 112 + j * 16 + fg * 4);
    SB();
#pragma unroll
    for (int p = 0; p < A_XW; ++p) { xb[p][0] = xp[p * 64]; xb[p][1] = xp[(18 + p) * 64]; SB(); }
#pragma unroll
    for (int mt = 0; mt < 2; ++mt) ln_comb2(st[mt], mean[mt], rstd[mt]);
#pragma unroll
    for (int j = 0; j < 7; ++j) { acc[j][0] = cs[j] * (-mean[0]); acc[j][1] = cs[j] * (-mean[1]); }
    SB();
  }
  s8 wf[7], wp[5];
  __builtin_amdgcn_s_setprio(A_PRIO);
  BAR0();
  ST(1, 1); CY0(1);
#pragma unroll
  for (int j = 0; j < 7; ++j) wf[j] = wl[j * 64];
  SB();
#pragma unroll
  for (int u = 0; u < 18; ++u) {
    const bool rd = u + 1 < 18;
    if (rd && (u & 1)) BAR0();
#pragma unroll
    for (int j = 0; j < 7; ++j) {
      mfma_acc(acc[j][0], wf[j], xb[u][0]);
      SB();
      if (j > 0) { if (rd) wf[j - 1] = wrd((u + 1) * 7 + j - 1); }
      else if (u > 0) wf[6] = wrd(u * 7 + 6);
      SB();
      mfma_acc(acc[j][1], wf[j], xb[u][1]);
      SB();
    }
    if (u + A_XW < 18) { xb[u + A_XW][0] = xp[(u + A_XW) * 64]; xb[u + A_XW][1] = xp[(18 + u + A_XW) * 64]; SB(); }
    if (u == 10) {
#pragma unroll
      for (int f = 0; f < 5; ++f) wp[f] = ((const s8*)wphiF)[(hp * 8 + (f < 4 ? f + 2 : 7)) * 64 + lane];
      SB();
    }
  }
  MFMA_FENCE();
  SB();
  ST(1, 2); CY1(1);
  const f4 z4 = {0, 0, 0, 0};
  AS3 char* stg = (AS3 char*)smem + w * 10240;
  auto pkc = [&](int j, int mt, int r) { return cvtpk((f2){acc[j][mt][2 * r], acc[j][mt][2 * r + 1]} * rstd[mt]); };
  u2 t[2];
  {
    AS3 u4* ho = (AS3 u4*)(smem + A_HO + w * 3072) + lane;
#pragma unroll
    for (int mt = 0; mt < 2; ++mt) {
      ho[mt * 64] = (u4){pkc(0, mt, 0), pkc(0, mt, 1), pkc(1, mt, 0), pkc(1, mt, 1)};
      t[mt] = (u2){pkc(6, mt, 0), pkc(6, mt, 1)};
    }
    ho[128] = (u4){t[0][0], t[0][1], t[1][0], t[1][1]};
    asm volatile("s_waitcnt lgkmcnt(0)" ::: "memory");
    if (lane == 0) *(AS3 unsigned*)(smem + A_FL + w * 4) = 1u;
    SB();
  }
  {
    u4 m[2], om[4]; u2 ot;
#pragma unroll
    for (int mt = 0; mt < 2; ++mt) m[mt] = (u4){pkc(2, mt, 0), pkc(2, mt, 1), pkc(3, mt, 0), pkc(3, mt, 1)};
#pragma unroll
    for (int mt = 0; mt < 2; ++mt) {
      AS3 char* row = stg + (mt * 16 + fi) * 160;
#pragma unroll
      for (int hh = 0; hh < 2; ++hh) {
        *(AS3 u2*)(row + hh * 5120 + 80 + fg * 8) = (u2){pkc(4 + hh, mt, 0), pkc(4 + hh, mt, 1)};
        if (fg == 2) *(AS3 u2*)(row + hh * 5120 + 112) = (u2){t[mt][hh], 0x00003f80u};
      }
    }
    phi4(wp, m, t, om, ot);
#pragma unroll
    for (int b = 0; b < 4; ++b) *(AS3 u4*)(stg + (b & 1) * 5120 + ((b >> 1) * 16 + fi) * 160 + fg * 16) = om[b];
    *(AS3 u2*)(stg + (fg & 1) * 5120 + ((fg >> 1) * 16 + fi) * 160 + 64) = ot;
  }
  ST(1, 3);
  {
    AS3 float* img = (AS3 float*)(smem + 40 * 1024) + w * 1536;
#pragma unroll
    for (int hh = 0; hh < 2; ++hh) {
      const AS3 char* bk = stg + hh * 5120 + (4 * fg + (fi >> 2)) * 160 + (fi & 3) * 8;
      s8 bv[2];
#pragma unroll
      for (int nt = 0; nt < 2; ++nt) bv[nt] = __builtin_shufflevector(trread(bk + 80 + nt * 32), trread(bk + 80 + nt * 32 + 16 * 160), 0, 1, 2, 3, 4, 5, 6, 7);
#pragma unroll
      for (int mt = 0; mt < 3; ++mt) {
        s8 a = __builtin_shufflevector(trread(bk + mt * 32), trread(bk + mt * 32 + 16 * 160), 0, 1, 2, 3, 4, 5, 6, 7);
        f4 k0 = mfma32(a, bv[0], z4), k1 = mfma32(a, bv[1], z4);
        if (mt < 2) {
          *(AS3 f4*)(img + hh * 512 + (fg & 1) * 256 + ((2 * mt + (fg >> 1)) * 16 + fi) * 4) = k0;
          if (fi < 3) *(AS3 f4*)(img + 1152 + (fg & 1) * 128 + (2 * mt + (fg >> 1)) * 32 + hh * 16 + fi * 4) = k1;
        } else if (fg == 0) {
          *(AS3 f4*)(img + 1024 + hh * 64 + fi * 4) = k0;
          if (fi < 3) *(AS3 f4*)(img + 1408 + hh * 16 + fi * 4) = k1;
        }
      }
    }
  }
  ST(1, 4);
  asm volatile("s_waitcnt lgkmcnt(0)" ::: "memory");
  if (lane == 0) __hip_atomic_fetch_add((AS3 unsigned*)(smem + A_FL + 16), 1u, __ATOMIC_RELAXED, __HIP_MEMORY_SCOPE_WORKGROUP);
  lds_wait_ge(lds_addr(smem) + A_FL + 16, 4);
  {
    const AS3 float* kp = (const AS3 float*)(smem + 40 * 1024);
    float* gp = kvacc + (hp >> 1) * KVQ;
    const int par = hp & 1;
#pragma unroll
    for (int i = 0; i < 6; ++i) {
      const int a = i * 256 + tid;
      int go; bool ok = true;
      if (i < 4) go = par * 1024 + a;
      else if (a < 1152) go = 2048 + par * 128 + (a - 1024);
      else if (a < 1408) { const int q = a - 1152; go = 2304 + (q >> 5) * 64 + par * 32 + (q & 31); ok = (q & 12) != 12; }
      else { const int q = a - 1408, g = par * 2 + ((q >> 4) & 1); go = 2816 + g * 80 + (q & 15); ok = q < 32 && (q & 12) != 12; }
      const float v = (kp[a] + kp[a + 1536]) + (kp[a + 3072] + kp[a + 4608]);
      if (ok) unsafeAtomicAdd(gp + go, v);
    }
  }
  ST(1, 5);
  ST(1, 6);
  ST_OUTP(1);
}

DI float smul(float a, float b) { float r = a * b; asm("" : "+v"(r)); return r; }
typedef _Float16 h2 __attribute__((ext_vector_type(2)));
DI unsigned cvtph(f2 v) { return __builtin_bit_cast(unsigned, __builtin_convertvector(v, h2)); }
DI f2 unph(unsigned u) { return __builtin_convertvector(__builtin_bit_cast(h2, u), f2); }

#ifndef M_PD
#define M_PD 4
#endif
#ifndef M_PRIO
#define M_PRIO 2
#endif
#ifndef M_NS
#define M_NS 2
#endif
#ifndef M_WARM
#define M_WARM 0
#endif
#ifndef M_ST
#define M_ST 0
#endif
#ifndef M_P0
#define M_P0 1
#define M_P1 2
#endif
#ifndef M_X
#define M_X 0
#endif
#ifndef M_FLAG
#define M_FLAG 1
#endif
#define M_CB (2 * 73728)
#define M_SS (M_CB + 5120)
#define M_FL (M_SS + 4096)
#ifndef M_TCH
#define M_TCH 0
#endif
DI void mfma_z(f4& c, s8 a, s8 b) { asm volatile("v_mfma_f32_16x16x32_bf16 %0, %1, %2, 0" : "=v"(c) : "v"(a), "v"(b)); }
DI float sfma(float a, float b, float c) { float r = __builtin_fmaf(a, b, c); asm("" : "+v"(r)); return r; }
__global__ __launch_bounds__(768, 3) void kM(const bf16* __restrict__ xF, const bf16* __restrict__ wM, const float* __restrict__ cb1,
                                             const float* __restrict__ stats, unsigned* __restrict__ part) {
  __shared__ __attribute__((aligned(16))) char smem[M_FL + 64];
  const int tid = threadIdx.x, lane = tid & 63, w = __builtin_amdgcn_readfirstlane(tid >> 6);
  const int xcd = blockIdx.x & 7, li = blockIdx.x >> 3;
  const int slice = (xcd % (8 / M_NS)) * M_NS + li % M_NS, rt = (xcd / (8 / M_NS)) * (32 / M_NS) + li / M_NS;
  const int fi = lane & 15, fg = lane >> 4;
  const unsigned l16 = lane * 16;
  ST_DECL ST(3, 0); ST(4, 0);
  if (w < 7) {
    const char* src = w < 5 ? (const char*)cb1 + slice * 4608 + w * 1024 : (const char*)stats + rt * 2048 + (w - 5) * 1024;
    glds16a(src + l16, lds_addr(smem) + M_CB + w * 1024);
  } else if (w == 9) ((AS3 unsigned*)(smem + M_FL))[lane & 15] = 0u;
  SB();
  const char* xsrc = (const char*)xF + (size_t)rt * 72 * 1024 + l16;
  auto xdma = [&](int i) { const int p = i * 12 + w; glds16a(xsrc + ((p & 3) * 18 + (p >> 2)) * 1024, lds_addr(smem) + p * 1024); };
  xdma(0); xdma(1);
  ST(4, 1);
  BAR0();
  SB();
  ST(4, 2);
  const char* wbase = (const char*)wM + (size_t)(slice * 12 + w) * 108 * 1024;
  auto wld = [&](int u, int t) { return *(const s8*)(wbase + (u * 3 + t) * 1024 + l16); };
  constexpr int TG = 9 * M_NS;
  const unsigned toff = ((li / M_NS) * TG + (lane < TG ? lane : 0)) * 64;
  unsigned tch = 0;
  auto touch = [&](int c) { if (M_TCH) { asm volatile("" :: "v"(tch)); tch = *(const unsigned*)(wbase + c * 18432 + toff); SB(); } };
  s8 wb[M_PD][3];
#pragma unroll
  for (int t = 0; t < 3; ++t) { wb[0][t] = wld(0, t); SB(); }
  xdma(2); xdma(3);
#pragma unroll
  for (int t = 0; t < 3; ++t) { wb[1][t] = wld(1, t); SB(); }
  __builtin_amdgcn_s_setprio(3);
  ST(4, 3);
  asm volatile("s_waitcnt vmcnt(8) lgkmcnt(0)\n\ts_barrier" ::: "memory");
  ST(4, 4); ST(3, 1);
  xdma(4); xdma(5);
#pragma unroll
  for (int t = 0; t < 3; ++t) { wb[2][t] = wld(2, t); SB(); }
  f4 acc[3][4];
  float r2[4];
  {
    f4 cs[3], bb[3];
#pragma unroll
    for (int t = 0; t < 3; ++t) { const AS3 f4* p = (const AS3 f4*)(smem + M_CB + ((w * 3 + t) * 4 + fg) * 32); cs[t] = p[0]; bb[t] = p[1]; }
    f4 st[2];
#pragma unroll
    for (int q = 0; q < 2; ++q) st[q] = *(const AS3 f4*)(smem + M_SS + lane * 32 + q * 16);
    const float mean = ((st[0][0] + st[0][2]) + (st[1][0] + st[1][2])) * 0.25f;
    const float e0 = st[0][0] - mean, e1 = st[0][2] - mean, e2 = st[1][0] - mean, e3 = st[1][2] - mean;
    const float var = (((st[0][1] + st[0][3]) + (st[1][1] + st[1][3])) + 144.f * ((e0 * e0 + e1 * e1) + (e2 * e2 + e3 * e3))) * (1.0f / 576.0f) + 1e-5f;
    const float rstd = __builtin_amdgcn_rsqf(var), sd = var * rstd, rr = rstd * rstd;
#pragma unroll
    for (int tt = 0; tt < 4; ++tt) {
      const float nm = -__shfl(mean, tt * 16 + fi), sg = __shfl(sd, tt * 16 + fi);
      r2[tt] = __shfl(rr, tt * 16 + fi);
#pragma unroll
      for (int t = 0; t < 3; ++t) {
#pragma unroll
        for (int r = 0; r < 4; ++r) acc[t][tt][r] = sfma(cs[t][r], nm, smul(bb[t][r], sg));
        asm volatile("" : "+v"(acc[t][tt]));
      }
    }
  }
  const AS3 s8* l0 = (const AS3 s8*)smem + lane;
  const AS3 s8* l1 = opaque_lds(l0 + 4096);
  const AS3 s8* l2 = opaque_lds(l0 + 8192);
  auto lrd = [&](int piece) { return piece < 64 ? l0[piece * 64] : piece < 128 ? l1[(piece - 64) * 64] : l2[(piece - 128) * 64]; };
  const unsigned fla = lds_addr(smem) + M_FL;
  SB();
  ST(4, 5);
  s8 xf[2][4];
#pragma unroll
  for (int ph = 0; ph < 2; ++ph) {
#pragma unroll
    for (int tt = 0; tt < 4; ++tt) xf[0][tt] = lrd(ph * 72 + tt);
    SB();
    if (ph == 0) {
#pragma unroll
      for (int p = 3; p < M_PD; ++p)
#pragma unroll
        for (int t = 0; t < 3; ++t) { wb[p][t] = wld(p, t); SB(); }
      touch(1);
    }
#pragma unroll
    for (int ks = 0; ks < 18; ++ks) {
      const int u = ph * 18 + ks;
      if (M_PRIO == 2) {
        if (ks == 5) __builtin_amdgcn_s_setprio(2);
        if (ks == 10) __builtin_amdgcn_s_setprio(1);
        if (ks == 14) __builtin_amdgcn_s_setprio(0);
      }
      if (u == 5) { lds_wait_ge(fla, 12); SB(); }
#pragma unroll
      for (int t = 0; t < 3; ++t) {
#pragma unroll
        for (int tt = 0; tt < 4; ++tt) {
          if (u == 18) mfma_z(acc[t][tt], wb[u % M_PD][t], xf[ks & 1][tt]);
          else mfma_acc(acc[t][tt], wb[u % M_PD][t], xf[ks & 1][tt]);
          SB();
          if (t == 0 && ks + 1 < 18) { xf[(ks + 1) & 1][tt] = lrd(ph * 72 + (ks + 1) * 4 + tt); SB(); }
        }
        if (u == 2 && t == 0) {
          if (lane == 0) __hip_atomic_fetch_add((AS3 unsigned*)(smem + M_FL), 1u, __ATOMIC_RELAXED, __HIP_MEMORY_SCOPE_WORKGROUP);
          SB();
        }
        if (u + M_PD < 36) { wb[u % M_PD][t] = wld(u + M_PD, t); SB(); }
      }
      if (u % 6 == 4 && u / 6 + 2 < 6) touch(u / 6 + 2);
    }
    MFMA_FENCE();
    SB();
    if (ph == 0) {
      ST(3, 2);
      AS3 char* hb = (AS3 char*)smem + 73728 + ((3 * w) >> 1) * 4096 + lane * 16;
#pragma unroll
      for (int tt = 0; tt < 4; ++tt) {
        unsigned pk[3][2];
#pragma unroll
        for (int t = 0; t < 3; ++t)
#pragma unroll
          for (int r = 0; r < 2; ++r) {
            const float m0 = max2f(acc[t][tt][2 * r], 0.f), m1 = max2f(acc[t][tt][2 * r + 1], 0.f);
            pk[t][r] = cvtpk((f2){smul(m0, m0), smul(m1, m1)});
          }
        if (w & 1) {
          *(AS3 u2*)(hb + tt * 1024 + 8) = (u2){pk[0][0], pk[0][1]};
          *(AS3 u4*)(hb + tt * 1024 + 4096) = (u4){pk[1][0], pk[1][1], pk[2][0], pk[2][1]};
        } else {
          *(AS3 u4*)(hb + tt * 1024) = (u4){pk[0][0], pk[0][1], pk[1][0], pk[1][1]};
          *(AS3 u2*)(hb + tt * 1024 + 4096) = (u2){pk[2][0], pk[2][1]};
        }
      }
      SB();
      if (M_PRIO == 2) __builtin_amdgcn_s_setprio(3);
      BARRIER();
      SB();
      ST(3, 3); CY0(3);
    }
  }
  asm volatile("" :: "v"(tch));
  ST(3, 4); CY1(3);
  {
    char* pb = (char*)part + ((size_t)(slice * 128 + rt * 4) * 18 + ((3 * w) >> 1)) * 1024;
#pragma unroll
    for (int tt = 0; tt < 4; ++tt) {
      unsigned pk[3][2];
#pragma unroll
      for (int t = 0; t < 3; ++t)
#pragma unroll
        for (int r = 0; r < 2; ++r) pk[t][r] = cvtph((f2){smul(acc[t][tt][2 * r], r2[tt]), smul(acc[t][tt][2 * r + 1], r2[tt])});
      char* q = pb + tt * 18 * 1024;
      if (w & 1) {
        st_wt(q + 512 + lane * 8, (u2){pk[0][0], pk[0][1]}); st_wt(q + 1024 + lane * 16, (u4){pk[1][0], pk[1][1], pk[2][0], pk[2][1]});
      } else {
        st_wt(q + lane * 16, (u4){pk[0][0], pk[0][1], pk[1][0], pk[1][1]}); st_wt(q + 1024 + lane * 8, (u2){pk[2][0], pk[2][1]});
      }
    }
  }
  ST(3, 5);
  ST(3, 6);
  ST_OUT(3); ST_OUT(4);
}

template <bool FINAL>
__global__ __launch_bounds__(576) void kR(const unsigned* __restrict__ part, const bf16* __restrict__ b2, const bf16* __restrict__ xresF,
                                          bf16* __restrict__ xout, float* __restrict__ statsA) {
  __shared__ float red[9 * 16 * 2];
  const int tid = threadIdx.x, lane = tid & 63, w = __builtin_amdgcn_readfirstlane(tid >> 6);
  const int tt = blockIdx.x >> 1, hf = blockIdx.x & 1, ks = hf * 9 + w, unit = tt * 18 + ks;
  const int fi = lane & 15, fg = lane >> 4;
  u4 p[8];
  const char* pb = (const char*)part + (size_t)unit * 1024;
  if (w % 3 == 1) {
#pragma unroll
    for (int s = 0; s < 8; ++s) {
      const u2 a = *(const u2*)(pb + (size_t)s * 2304 * 1024 + lane * 8), b = *(const u2*)(pb + (size_t)s * 2304 * 1024 + 512 + lane * 8);
      p[s] = (u4){a[0], a[1], b[0], b[1]};
    }
  } else {
#pragma unroll
    for (int s = 0; s < 8; ++s) p[s] = *(const u4*)(pb + (size_t)s * 2304 * 1024 + lane * 16);
  }
  const u4 xr = ((const u4*)xresF)[unit * 64 + lane];
  const u4 bv = *(const u4*)(b2 + ks * 32 + fg * 8);
  u4 o;
  f2 v[4];
  f2 s2 = {0.f, 0.f};
#pragma unroll
  for (int c = 0; c < 4; ++c) {
    f2 a = ((unph(p[0][c]) + unph(p[1][c])) + (unph(p[2][c]) + unph(p[3][c]))) + ((unph(p[4][c]) + unph(p[5][c])) + (unph(p[6][c]) + unph(p[7][c])));
    a = unpk(cvtpk(a)) + unpk(bv[c]);
    a = unpk(cvtpk(a)) + unpk(xr[c]);
    o[c] = cvtpk(a);
    v[c] = unpk(o[c]);
    s2 += v[c];
  }
  if (FINAL) {
    *(u4*)(xout + (size_t)(tt * 16 + fi) * D_ + ks * 32 + fg * 8) = o;
    return;
  }
  ((u4*)xout)[unit * 64 + lane] = o;
  const float m = red_sum(s2[0] + s2[1]) * (1.0f / 32.0f);
  f2 q2 = {0.f, 0.f};
#pragma unroll
  for (int c = 0; c < 4; ++c) { const f2 d = v[c] - m; q2 += d * d; }
  const float m2 = red_sum(q2[0] + q2[1]);
  if (fg == 0) { red[(w * 16 + fi) * 2] = m; red[(w * 16 + fi) * 2 + 1] = m2; }
  __syncthreads();
  if (tid < 16) {
    float mm[9], sm = 0.f, sq = 0.f;
#pragma unroll
    for (int i = 0; i < 9; ++i) { mm[i] = red[(i * 16 + tid) * 2]; sm += mm[i]; sq += red[(i * 16 + tid) * 2 + 1]; }
    sm *= (1.0f / 9.0f);
    float dq = 0.f;
#pragma unroll
    for (int i = 0; i < 9; ++i) { const float d = mm[i] - sm; dq += d * d; }
    float2 r; r.x = sm; r.y = sq + 32.f * dq;
    *(float2*)(statsA + ((tt * 16 + tid) * 2 + hf) * 2) = r;
  }
}

#define B_EB 1
#define B_PL 1
#define B_WR 2
#define B_W0 8
DI s8 cv8(f4 a, f4 b) { return mk8(cvtpk((f2){a[0], a[1]}), cvtpk((f2){a[2], a[3]}), cvtpk((f2){b[0], b[1]}), cvtpk((f2){b[2], b[3]})); }
#define B_FL (36 * 1024 + 9 * 32 * 8)
template <bool PREP>
__global__ __launch_bounds__(PREP ? 768 : 576, 3) void kB(const bf16* __restrict__ phiQ, const bf16* __restrict__ phiQt, const float* __restrict__ kvacc,
                                             const bf16* __restrict__ woF, const bf16* __restrict__ xresF, bf16* __restrict__ xout, float* __restrict__ stats, PREP_PARAMS) {
  const PrepArgs pa = {p_w1, p_w2, p_wo, p_wM, p_woF, p_cb1};
  __shared__ __attribute__((aligned(16))) char smem[B_FL + 64 + (PREP ? 3 * 8 * PFS * 16 : 0)];
  const int tid = threadIdx.x, lane = tid & 63, w = __builtin_amdgcn_readfirstlane(tid >> 6);
  const int xcd = blockIdx.x & 7, li = blockIdx.x >> 3;
  const int rt = xcd * 8 + (li >> 2), ct = li & 3;
  const int fi = lane & 15, fg = lane >> 4;
  ST_DECL ST(2, 0);
  if (PREP && w >= 9) {
    const int u = 768 + blockIdx.x * 3 + (w - 9);
    if (u >= PU_END) return;
    u4 r[8];
    BAR0();
    if (!B_PL) { prep_load(pa, u, lane, r); SB(); }
    BAR0();
    if (B_PL) { lds_wait_ge(lds_addr(smem) + B_FL + 4, 9); prep_load(pa, u, lane, r); SB(); }
    ST(2, 2);
    asm volatile("" :: "v"(r[7]));
    ST(2, 3);
    prep_finish(pa, u, lane, r, (AS3 u4*)(smem + B_FL + 64) + (w - 9) * 8 * PFS);
    ST(2, 5);
    ST(2, 6);
    ST_OUTP(2);
    return;
  }
  const s8* wsrc = (const s8*)woF + (size_t)(ct * 9 + w) * 18 * 64 + lane;
  s8 wf[18];
  const int T = ct * 9 + w;
  const size_t xo = ((size_t)(rt * 2 * 18 + (T >> 1)) * 64 + ((T & 1) * 2 + (fg >> 1)) * 16 + fi) * 16 + (fg & 1) * 8;
  u2 xr[2];
  if (w < 8) {
    const f4* kp = (const f4*)(kvacc + w * KVQ) + lane;
    const s8* pp = (const s8*)phiQ + ((size_t)rt * 2 * 32 + w * 4) * 64 + lane;
    f4 a0[4][2], a1, a2[2], a3 = {0.f, 0.f, 0.f, 0.f};
    s8 pb[2][4];
    a1 = kp[512]; a2[0] = kp[576]; a2[1] = kp[640];
    if ((fi >> 2) == fg) a3 = kp[704];
    const s8 pt = ((const s8*)phiQt)[((size_t)rt * 8 + w) * 64 + lane];
    SB();
#pragma unroll
    for (int m = 0; m < 4; ++m) { a0[m][0] = kp[m * 128]; a0[m][1] = kp[m * 128 + 64]; pb[0][m] = pp[m * 64]; pb[1][m] = pp[(32 + m) * 64]; SB(); }
    if (B_EB) BAR0();
#pragma unroll
    for (int s = 0; s < B_W0; ++s) { wf[s] = wsrc[s * 64]; SB(); }
    const f4 z4 = {0, 0, 0, 0};
    const unsigned t1[2] = {cvtpk((f2){a1[0], a1[1]}), cvtpk((f2){a1[2], a1[3]})};
    const bool c3 = (fi & 3) == 3;
    const unsigned t3[2] = {c3 ? 0u : cvtpk((f2){a3[0], a3[1]}), c3 ? 0u : cvtpk((f2){a3[2], a3[3]})};
    const u4 f2a = __builtin_bit_cast(u4, cv8(a2[0], a2[1]));
    f4 om[2][4], otl[2];
    otl[0] = mfma32(mk8(t3[0], t3[1], 0u, 0u), pt, z4);
    otl[1] = mfma32(mk8(0u, 0u, t3[0], t3[1]), pt, z4);
#pragma unroll
    for (int m = 0; m < 4; ++m) {
      const bool s1 = fg == m, s2 = (fi >> 2) == m && !c3;
      const unsigned g0 = s1 ? t1[0] : 0u, g1 = s1 ? t1[1] : 0u;
      om[0][m] = mfma32(mk8(g0, g1, 0u, 0u), pt, z4);
      om[1][m] = mfma32(mk8(0u, 0u, g0, g1), pt, z4);
      const s8 f2_ = mk8(s2 ? f2a[0] : 0u, s2 ? f2a[1] : 0u, s2 ? f2a[2] : 0u, s2 ? f2a[3] : 0u);
      const s8 f0 = cv8(a0[m][0], a0[m][1]);
#pragma unroll
      for (int tt = 0; tt < 2; ++tt) { otl[tt] = mfma32(f2_, pb[tt][m], otl[tt]); om[tt][m] = mfma32(f0, pb[tt][m], om[tt][m]); }
    }
#pragma unroll
    for (int tt = 0; tt < 2; ++tt) {
      const float invs = __builtin_amdgcn_rcpf(otl[tt][2] + 1e-6f);
      float inv[4];
      agath(invs, inv);
      unsigned po[4][2];
#pragma unroll
      for (int m = 0; m < 4; ++m) { po[m][0] = cvtpk((f2){om[tt][m][0], om[tt][m][1]} * inv[m]); po[m][1] = cvtpk((f2){om[tt][m][2], om[tt][m][3]} * inv[m]); }
      AS3 char* ol = (AS3 char*)smem + tt * 18 * 1024 + lane * 16;
      *(AS3 u4*)(ol + (2 * w) * 1024) = (u4){po[0][0], po[0][1], po[1][0], po[1][1]};
      *(AS3 u4*)(ol + (2 * w + 1) * 1024) = (u4){po[2][0], po[2][1], po[3][0], po[3][1]};
      *(AS3 unsigned*)(ol + (16 + (w >> 2)) * 1024 + (w & 3) * 4) = cvtpk((f2){otl[tt][0], otl[tt][1]} * invs);
    }
  } else {
    if (lane < 2) *(AS3 unsigned*)(smem + B_FL + lane * 4) = 0u;
    if (B_EB) BAR0();
#pragma unroll
    for (int s = 0; s < B_W0; ++s) { wf[s] = wsrc[s * 64]; SB(); }
  }
  ST(2, 1);
  BARRIER();
  ST(2, 2);
  f4 acc[2] = {{0, 0, 0, 0}, {0, 0, 0, 0}};
  {
    const AS3 s8* ol = (const AS3 s8*)smem + lane;
    s8 ob[2][2];
    ob[0][0] = ol[0]; ob[0][1] = ol[18 * 64];
#pragma unroll
    for (int s = 0; s < 18; ++s) {
#pragma unroll
      for (int q = B_W0 + s * B_WR; q < B_W0 + (s + 1) * B_WR; ++q) {
        if (q < 18) wf[q] = wsrc[q * 64];
        else if (q < 20) xr[q - 18] = *(const u2*)((const char*)xresF + xo + (q - 18) * 18 * 1024);
      }
      if (PREP && B_PL && s == (19 - B_W0) / B_WR) { SB(); if (lane == 0) __hip_atomic_fetch_add((AS3 unsigned*)(smem + B_FL + 4), 1u, __ATOMIC_RELAXED, __HIP_MEMORY_SCOPE_WORKGROUP); }
      if (s + 1 < 18) { ob[(s + 1) & 1][0] = ol[(s + 1) * 64]; ob[(s + 1) & 1][1] = ol[(19 + s) * 64]; }
      SB();
      acc[0] = mfma32(wf[s], ob[s & 1][0], acc[0]);
      acc[1] = mfma32(wf[s], ob[s & 1][1], acc[1]);
      SB();
    }
  }
  ST(2, 3);
  AS3 float* red = (AS3 float*)(smem + 36 * 1024);
  u2 o[2];
#pragma unroll
  for (int tt = 0; tt < 2; ++tt)
#pragma unroll
    for (int c = 0; c < 2; ++c) o[tt][c] = cvtpk(unpk(cvtpk((f2){acc[tt][2 * c], acc[tt][2 * c + 1]})) + unpk(xr[tt][c]));
  SB();
#pragma unroll
  for (int tt = 0; tt < 2; ++tt) st_wt((char*)xout + xo + tt * 18 * 1024, o[tt]);
#pragma unroll
  for (int tt = 0; tt < 2; ++tt) {
    const f2 v[2] = {unpk(o[tt][0]), unpk(o[tt][1])};
    const float m = red_sum((v[0][0] + v[0][1]) + (v[1][0] + v[1][1])) * (1.0f / 16.0f);
    const f2 d0 = v[0] - m, d1 = v[1] - m;
    const float m2 = red_sum((d0[0] * d0[0] + d0[1] * d0[1]) + (d1[0] * d1[0] + d1[1] * d1[1]));
    if (fg == 0) *(AS3 f2*)(red + ((tt * 16 + fi) * 9 + w) * 2) = (f2){m, m2};
  }
  asm volatile("s_waitcnt lgkmcnt(0)" ::: "memory");
  if (w > 0) {
    if (lane == 0) __hip_atomic_fetch_add((AS3 unsigned*)(smem + B_FL), 1u, __ATOMIC_RELAXED, __HIP_MEMORY_SCOPE_WORKGROUP);
  } else lds_wait_ge(lds_addr(smem) + B_FL, 8);
  if (tid < 32) {
    float mm[9], sm = 0.f, sq = 0.f;
#pragma unroll
    for (int i = 0; i < 9; ++i) { const f2 p = *(const AS3 f2*)(red + (tid * 9 + i) * 2); mm[i] = p[0]; sm += p[0]; sq += p[1]; }
    sm *= (1.0f / 9.0f);
    float dq = 0.f;
#pragma unroll
    for (int i = 0; i < 9; ++i) { const float d = mm[i] - sm; dq += d * d; }
    *(f2*)(stats + ((rt * 32 + tid) * 4 + ct) * 2) = (f2){sm, sq + 16.f * dq};
  }
  ST(2, 4);
  ST_OUTP(2);
}

typedef unsigned u4a __attribute__((ext_vector_type(4), aligned(4)));
#define PDRAIN()
#define PT 576
#define PB_QKV 27
#define PB_X 128
#define PB_PHI 32
#define PB_Z 24
#define PB_TOTAL (PB_QKV + PB_X + PB_PHI + PB_Z)
__global__ __launch_bounds__(PT) void kP(const bf16* __restrict__ x0, const bf16* __restrict__ wqkv, const bf16* __restrict__ wphi, const bf16* __restrict__ b1,
                                          bf16* __restrict__ x0F, float* __restrict__ stats, bf16* __restrict__ wqkvF, float* __restrict__ csA,
                                          bf16* __restrict__ wphiF, float* __restrict__ cb1, float* __restrict__ kvacc) {
  __shared__ float red[9 * 64];
  __shared__ u4 stg[16 * PFS];
  int b = blockIdx.x;
  const int t = threadIdx.x, lane = t & 63, w = __builtin_amdgcn_readfirstlane(t >> 6);
  ST_DECL ST(5, 0);
  if (b < PB_QKV) {
    const int oc = lane & 7, kb = lane >> 3;
    const bf16* src = wqkv + (size_t)(w * 64 + kb * 8) * (3 * D_) + b * 64 + oc * 8;
    u4 r[8];
#pragma unroll
    for (int e = 0; e < 8; ++e) r[e] = ldnt(src + e * (3 * D_));
    float sm[8];
#pragma unroll
    for (int c = 0; c < 4; ++c) {
      const f2 p = ((unpk(r[0][c]) + unpk(r[1][c])) + (unpk(r[2][c]) + unpk(r[3][c]))) + ((unpk(r[4][c]) + unpk(r[5][c])) + (unpk(r[6][c]) + unpk(r[7][c])));
      sm[2 * c] = p[0]; sm[2 * c + 1] = p[1];
    }
    ST(5, 1);
    const bool b0 = kb & 1, b1_ = kb & 2, b2 = kb & 4;
    float v[4], u[2];
#pragma unroll
    for (int q = 0; q < 4; ++q) v[q] = (b0 ? sm[4 + q] : sm[q]) + __shfl_xor(b0 ? sm[q] : sm[4 + q], 8);
#pragma unroll
    for (int q = 0; q < 2; ++q) u[q] = (b1_ ? v[2 + q] : v[q]) + __shfl_xor(b1_ ? v[q] : v[2 + q], 16);
    red[w * 64 + oc * 8 + (b0 ? 4 : 0) + (b1_ ? 2 : 0) + (b2 ? 1 : 0)] = (b2 ? u[1] : u[0]) + __shfl_xor(b2 ? u[0] : u[1], 32);
    BARRIER();
    if (t < 64) {
      const int c = b * 64 + lane, th = c / 576, cc = c % 576, hd = cc / 18, i = cc % 18, j = th * 2 + (hd & 1);
      float s0 = 0.f;
#pragma unroll
      for (int k = 0; k < 9; ++k) s0 += red[k * 64 + t];
      float* q = csA + (hd >> 1) * 112 + (i < 16 ? j * 16 + i : 96 + 2 * j + i - 16);
      q[0] = s0;
      if (th == 2 && i >= 16) q[4] = 0.f;
    }
    ST(5, 2); PDRAIN(); ST(5, 3); ST_OUT(5);
    return;
  }
  b -= PB_QKV;
  if (b < PB_X) {
    if (w >= 4) return;
    if (w >= 2) {
      const int un = b * 2 + (w - 2);
      if (un >= 243) return;
      const int cb = un / 9, kq = un % 9;
      u4 r[8], o[8];
      u4* wstg = stg + (w - 2) * 8 * PFS;
      const int oc = lane & 7, kb = lane >> 3;
      const bf16* src = wqkv + (size_t)(kq * 64 + kb * 8) * (3 * D_) + cb * 64 + oc * 8;
#pragma unroll
      for (int e = 0; e < 8; ++e) r[e] = ldnt(src + e * (3 * D_));
      const int c = cb * 64 + lane, th = c / 576, cc = c % 576, hd = cc / 18, i = cc % 18, j = th * 2 + (hd & 1);
      u4* fd = (u4*)wqkvF + (size_t)(hd >> 1) * 126 * 64 + (kq * 2 * 7) * 64 + (i < 16 ? j * 64 + i : 6 * 64 + 2 * j + i - 16);
      if (th == 2 && i >= 16) {
#pragma unroll
        for (int f = 0; f < 8; ++f) fd[(f >> 2) * 7 * 64 + (f & 3) * 16 + 4] = (u4){0u, 0u, 0u, 0u};
      }
      tr88(r, o);
      ST(5, 1);
      u4* dst = wstg + kb * PFS + oc * 8;
#pragma unroll
      for (int n = 0; n < 8; ++n) dst[n] = o[n];
      WAVE_SYNC();
#pragma unroll
      for (int f = 0; f < 8; ++f) fd[(f >> 2) * 7 * 64 + (f & 3) * 16] = wstg[f * PFS + lane];
      ST(5, 2); PDRAIN(); ST(5, 3); ST_OUT(5);
      return;
    }
    const int tt = b, hf = w, fi = lane & 15, fg = lane >> 4;
    const bf16* src = x0 + (size_t)(tt * 16 + fi) * D_ + hf * 288 + fg * 8;
    u4 v[9];
#pragma unroll
    for (int q = 0; q < 9; ++q) v[q] = ldnt(src + q * 32);
    float sm = 0.f;
#pragma unroll
    for (int q = 0; q < 9; ++q) sm += sum8(v[q]);
    asm volatile("" : "+v"(sm));
    SB();
    ST(5, 1);
#pragma unroll
    for (int q = 0; q < 9; ++q) st_wt((u4*)x0F + (tt * 18 + hf * 9 + q) * 64 + lane, v[q]);
    const float m = red_sum(sm) * (1.0f / 288.0f);
    f2 m2 = {0.f, 0.f};
#pragma unroll
    for (int q = 0; q < 9; ++q)
#pragma unroll
      for (int c = 0; c < 4; ++c) { f2 d = unpk(v[q][c]) - m; m2 += d * d; }
    const float q2 = red_sum(m2[0] + m2[1]);
    float2 st; st.x = m; st.y = q2;
    if (fg == 0) *(float2*)(stats + ((tt * 16 + fi) * 2 + hf) * 2) = st;
    ST(5, 2); PDRAIN(); ST(5, 3); ST_OUT(5);
    return;
  }
  b -= PB_X;
  if (b < PB_PHI) {
    if (w >= 4) return;
    const int wu = b * 4 + w;
    const int f = wu & 7, hp = wu >> 3;
    const int i = lane & 15, g = lane >> 4;
    unsigned short val[8];
#pragma unroll
    for (int e = 0; e < 8; ++e) {
      int bb = 0, d = 0, ft = 0; bool ok = false;
      const int rho = 4 * g + (e & 3);
      if (f < 4) {
        bb = f; ft = i;
        if (e < 4) { d = rho; ok = true; }
        else { d = 16 + (rho & 1); ok = (rho >> 1) == bb; }
      } else {
        const int c = i & 3, sl = i >> 2;
        bb = sl; ft = 16 + (c & 1);
        if (f == 4) { d = rho; ok = bb == (e >> 2); }
        else if (f == 5) { d = rho; ok = bb == 2 + (e >> 2); }
        else { d = 16 + (rho & 1); ok = (e >> 2) == (sl >> 1) && (rho >> 1) == (f - 6) * 2 + (sl & 1); }
        ok = ok && c < 2;
      }
      unsigned short wv = __builtin_bit_cast(unsigned short, wphi[((2 * hp + (bb & 1)) * DH_ + d) * DH_ + ft]);
      val[e] = ok ? wv : (unsigned short)0;
    }
    u4 ov;
#pragma unroll
    for (int e = 0; e < 4; ++e) ov[e] = (unsigned)val[2 * e] | ((unsigned)val[2 * e + 1] << 16);
    ((u4*)wphiF)[wu * 64 + lane] = ov;
    ST(5, 2); PDRAIN(); ST(5, 3); ST_OUT(5);
    return;
  }
  b -= PB_PHI;
  {
    if (t < 512) ((f4*)kvacc)[b * 512 + t] = (f4){0.f, 0.f, 0.f, 0.f};
    else if (t < 560) {
      const int q = b * 48 + (t - 512);
      const u2 bv = *(const u2*)(b1 + q * 4);
      const f2 lo = unpk(bv[0]), hi = unpk(bv[1]);
      ((f4*)cb1)[q * 2] = (f4){0.f, 0.f, 0.f, 0.f};
      ((f4*)cb1)[q * 2 + 1] = (f4){lo[0], lo[1], hi[0], hi[1]};
    }
    ST(5, 2); PDRAIN(); ST(5, 3); ST_OUT(5);
  }
}

extern "C" void kernel_launch(void* const* d_in, const int* in_sizes, int n_in,
                              void* d_out, int out_size, void* d_ws, size_t ws_size,
                              hipStream_t stream) {
  const bf16* x0 = (const bf16*)d_in[0];
  const bf16* wqkv = (const bf16*)d_in[1];
  const bf16* wphi = (const bf16*)d_in[2];
  const bf16* wo = (const bf16*)d_in[3];
  const bf16* w1 = (const bf16*)d_in[5];
  const bf16* b1 = (const bf16*)d_in[6];
  const bf16* w2 = (const bf16*)d_in[7];
  const bf16* b2 = (const bf16*)d_in[8];
  char* ws = (char*)d_ws;
  size_t off = 0;
  auto alloc = [&](size_t bytes) { void* p = ws + off; off += (bytes + 255) & ~(size_t)255; return p; };
  bf16* x0F = (bf16*)alloc(S_ * D_ * 2);
  bf16* x1F = (bf16*)alloc(S_ * D_ * 2);
  bf16* x2F = (bf16*)alloc(S_ * D_ * 2);
  unsigned* part = (unsigned*)alloc((size_t)8 * S_ * D_ * 2);
  bf16* wM = (bf16*)alloc((size_t)2 * D_ * F_ * 2);
  bf16* wqkvF = (bf16*)alloc((size_t)16 * 126 * 1024 + 2048);
  bf16* woF = (bf16*)alloc((size_t)8 * 81 * 1024);
  bf16* wphiF = (bf16*)alloc((size_t)16 * 8 * 1024);
  bf16* phiQ = (bf16*)alloc((size_t)128 * 32 * 1024);
  bf16* phiQt = (bf16*)alloc((size_t)128 * 8 * 512);
  float* kvacc = (float*)alloc(2 * 32 * KV_STRIDE * 4);
  float* cb1 = (float*)alloc(F_ * 8 + 1024);
  float* csA = (float*)alloc(3 * 16 * 112 * 4);
  float* stats1 = (float*)alloc(S_ * 16 * 4 + 65536);
  float* stats = (float*)alloc(S_ * 16 * 4 + 65536);
  kP<<<PB_TOTAL, PT, 0, stream>>>(x0, wqkv, wphi, b1, x0F, stats, wqkvF, csA, wphiF, cb1, kvacc);
  const bf16* xin = x0F;
  for (int layer = 0; layer < 2; ++layer) {
    float* kv = kvacc + layer * 32 * KV_STRIDE;
    if (layer == 0) {
      kA<true><<<256, 704, 0, stream>>>(xin, wqkvF, csA, wphiF, stats, phiQ, phiQt, kv, w1, w2, wo, wM, woF, cb1);
      if (A_PU > 1) kB<false><<<256, 576, 0, stream>>>(phiQ, phiQt, kv, woF, xin, x1F, stats1, w1, w2, wo, wM, woF, cb1);
      else kB<true><<<256, 768, 0, stream>>>(phiQ, phiQt, kv, woF, xin, x1F, stats1, w1, w2, wo, wM, woF, cb1);
    } else {
      kA<false><<<256, 512, 0, stream>>>(xin, wqkvF, csA, wphiF, stats, phiQ, phiQt, kv, w1, w2, wo, wM, woF, cb1);
      kB<false><<<256, 576, 0, stream>>>(phiQ, phiQt, kv, woF, xin, x1F, stats1, w1, w2, wo, wM, woF, cb1);
    }
    kM<<<256, 768, 0, stream>>>(x1F, wM, cb1, stats1, part);
    if (layer == 0) kR<false><<<256, 576, 0, stream>>>(part, b2, x1F, x2F, stats);
    else kR<true><<<256, 576, 0, stream>>>(part, b2, x1F, (bf16*)d_out, stats);
    xin = x2F;
  }
}
```

```cpp
#include <hip/hip_runtime.h>
#include <hip/hip_bf16.h>
#include <stdint.h>

typedef __hip_bfloat16 bf16;
typedef short s4 __attribute__((ext_vector_type(4)));
typedef short s8 __attribute__((ext_vector_type(8)));
typedef float f4 __attribute__((ext_vector_type(4)));
typedef __bf16 bf8 __attribute__((ext_vector_type(8)));
typedef unsigned u4 __attribute__((ext_vector_type(4)));
typedef unsigned u2 __attribute__((ext_vector_type(2)));
#define AS3 __attribute__((address_space(3)))
#define AS1 __attribute__((address_space(1)))
#define DI __device__ __forceinline__

#define S_ 2048
#define D_ 576
#define H_ 32
#define DH_ 18
#define F_ 4608

DI float b2f(bf16 x) { return __bfloat162float(x); }
DI bf16 f2b(float x) { return __float2bfloat16(x); }
DI float rb(float x) { return b2f(f2b(x)); }
DI float bfu(unsigned short u) { return __uint_as_float(((unsigned)u) << 16); }
DI unsigned pack2(float a, float b) {
  bf16 x = f2b(a), y = f2b(b);
  return (unsigned)__builtin_bit_cast(unsigned short, x) | ((unsigned)__builtin_bit_cast(unsigned short, y) << 16);
}
typedef float f2 __attribute__((ext_vector_type(2)));
typedef short s2 __attribute__((ext_vector_type(2)));
typedef __bf16 bf2 __attribute__((ext_vector_type(2)));
DI unsigned cvtpk(f2 v) { return __builtin_bit_cast(unsigned, __builtin_convertvector(v, bf2)); }
DI f2 unpk(unsigned u) { f2 r; r[0] = __uint_as_float(u << 16); r[1] = __uint_as_float(u & 0xffff0000u); return r; }
DI f4 mfma32(s8 a, s8 b, f4 c) { return __builtin_amdgcn_mfma_f32_16x16x32_bf16(__builtin_bit_cast(bf8, a), __builtin_bit_cast(bf8, b), c, 0, 0, 0); }
DI void mfma_acc(f4& c, s8 a, s8 b) { asm volatile("v_mfma_f32_16x16x32_bf16 %0, %1, %2, %0" : "+v"(c) : "v"(a), "v"(b)); }
#define MFMA_FENCE() asm volatile("s_nop 7\n\ts_nop 3" ::: "memory")
#define LDS_R128(d, addr, off) asm volatile("ds_read_b128 %0, %1 offset:%2" : "=v"(d) : "v"(addr), "i"(off))
#define LDS_RTR(d, addr, off) asm volatile("ds_read_b64_tr_b16 %0, %1 offset:%2" : "=v"(d) : "v"(addr), "i"(off))
DI unsigned lds_addr(const void* p) { return (unsigned)(uintptr_t)(AS3 const char*)p; }
DI const AS3 s8* opaque_lds(const AS3 s8* p) { unsigned a = (unsigned)(uintptr_t)p; asm volatile("" : "+v"(a)); return (const AS3 s8*)(uintptr_t)a; }
DI void glds16(const void* g, char* l) { __builtin_amdgcn_global_load_lds((const AS1 unsigned*)g, (AS3 unsigned*)l, 16, 0, 0); }
DI void glds16a(const void* g, unsigned l) { asm volatile("s_mov_b32 m0, %1\n\ts_nop 0\n\tglobal_load_lds_dwordx4 %0, off" :: "v"(g), "s"(l) : "memory"); }
#define WAIT_V(n) asm volatile("s_waitcnt vmcnt(" #n ")" ::: "memory")

#define STM_BYTES 0
#define STM(i)
#define GST(i)
#define PST(i)
#define STM_OUT()

#define SB() __builtin_amdgcn_sched_barrier(0)
#define UNDEF(x) asm volatile("" : "=v"(x))
#define RELANE() do { asm volatile("v_mbcnt_lo_u32_b32 %0, -1, 0\n\tv_mbcnt_hi_u32_b32 %0, -1, %0" : "=v"(lane)); __builtin_assume(lane >= 0 && lane < 64); fi = lane & 15; fg = lane >> 4; l16 = lane * 16; } while (0)
#define BAR0() asm volatile("s_barrier" ::: "memory")
#define BARRIER() asm volatile("s_waitcnt lgkmcnt(0)\n\ts_barrier" ::: "memory")

DI void ln_comb(const f4 (&p)[4], float& mean, float& rstd);
DI void ln_stats(const float* __restrict__ stats, int tok, float& mean, float& rstd) {
  const f4* p = (const f4*)(stats + tok * 16);
  f4 q[4] = {p[0], p[1], p[2], p[3]};
  ln_comb(q, mean, rstd);
}
DI void ln_comb(const f4 (&p)[4], float& mean, float& rstd) {
  f4 a = p[0], b = p[1], c = p[2], d = p[3];
  float m = (a[0] + a[2] + b[0] + b[2] + c[0] + c[2] + d[0] + d[2]) * 0.125f;
  float m2 = a[1] + a[3] + b[1] + b[3] + c[1] + c[3] + d[1] + d[3];
  float q = 0.f, t;
  t = a[0] - m; q += t * t; t = a[2] - m; q += t * t; t = b[0] - m; q += t * t; t = b[2] - m; q += t * t;
  t = c[0] - m; q += t * t; t = c[2] - m; q += t * t; t = d[0] - m; q += t * t; t = d[2] - m; q += t * t;
  float var = (m2 + 72.f * q) * (1.0f / 576.0f);
  mean = m; rstd = 1.0f / sqrtf(var + 1e-5f);
}

DI void ln_comb_var(const f4 (&p)[4], float& mean, float& var) {
  f4 a = p[0], b = p[1], c = p[2], d = p[3];
  float m = ((a[0] + a[2]) + (b[0] + b[2]) + ((c[0] + c[2]) + (d[0] + d[2]))) * 0.125f;
  float m2 = (a[1] + a[3]) + (b[1] + b[3]) + ((c[1] + c[3]) + (d[1] + d[3]));
  float q = 0.f, t;
  t = a[0] - m; q += t * t; t = a[2] - m; q += t * t; t = b[0] - m; q += t * t; t = b[2] - m; q += t * t;
  t = c[0] - m; q += t * t; t = c[2] - m; q += t * t; t = d[0] - m; q += t * t; t = d[2] - m; q += t * t;
  mean = m; var = (m2 + 72.f * q) * (1.0f / 576.0f) + 1e-5f;
}
DI void ln_comb2(f4 a, float& mean, float& rstd) {
  const float m = (a[0] + a[2]) * 0.5f, dlt = a[0] - a[2];
  const float var = (a[1] + a[3] + 144.f * dlt * dlt) * (1.0f / 576.0f);
  mean = m; rstd = 1.0f / sqrtf(var + 1e-5f);
}
DI int qkv_col(int hp, int j, int i) {
  if (j < 6) return 576 * (j >> 1) + 18 * (2 * hp + (j & 1)) + i;
  if (i >= 12) return -1;
  int t = i >> 1;
  return 576 * (t >> 1) + 18 * (2 * hp + (t & 1)) + 16 + (i & 1);
}
DI int wo_k(int s, int g, int e) {
  if (s < 16) return 18 * (2 * s + (e >> 2)) + 4 * g + (e & 3);
  int q = 4 * (s - 16) + (e >> 1);
  return 18 * (4 * q + g) + 16 + (e & 1);
}
DI float red_max(float x) {
  auto a = __builtin_amdgcn_permlane16_swap(__float_as_uint(x), __float_as_uint(x), false, false);
  x = fmaxf(__uint_as_float(a[0]), __uint_as_float(a[1]));
  auto b = __builtin_amdgcn_permlane32_swap(__float_as_uint(x), __float_as_uint(x), false, false);
  return fmaxf(__uint_as_float(b[0]), __uint_as_float(b[1]));
}
DI float red_min(float x) {
  auto a = __builtin_amdgcn_permlane16_swap(__float_as_uint(x), __float_as_uint(x), false, false);
  x = fminf(__uint_as_float(a[0]), __uint_as_float(a[1]));
  auto b = __builtin_amdgcn_permlane32_swap(__float_as_uint(x), __float_as_uint(x), false, false);
  return fminf(__uint_as_float(b[0]), __uint_as_float(b[1]));
}
DI float red_sum(float x) {
  auto a = __builtin_amdgcn_permlane16_swap(__float_as_uint(x), __float_as_uint(x), false, false);
  x = __uint_as_float(a[0]) + __uint_as_float(a[1]);
  auto b = __builtin_amdgcn_permlane32_swap(__float_as_uint(x), __float_as_uint(x), false, false);
  return __uint_as_float(b[0]) + __uint_as_float(b[1]);
}
DI s8 mk8(unsigned a, unsigned b, unsigned c, unsigned d) { u4 v = {a, b, c, d}; return __builtin_bit_cast(s8, v); }
DI s8 zx8(s4 a) { return __builtin_shufflevector(a, (s4){0, 0, 0, 0}, 0, 1, 2, 3, 4, 5, 6, 7); }
DI s4 trread(const AS3 char* p) { return __builtin_amdgcn_ds_read_tr16_b64_v4i16((AS3 s4*)p); }
#define KV_STRIDE 768
template <int V> struct IC { enum { value = V }; };

DI float max2f(float a, float b) { return __builtin_elementwise_maximum(a, b); }
DI float min2f(float a, float b) { return __builtin_elementwise_minimum(a, b); }
DI float max3f(float a, float b, float c) { return max2f(max2f(a, b), c); }
DI float min3f(float a, float b, float c) { return min2f(min2f(a, b), c); }
DI void swap16(float& a, float& b) { auto r = __builtin_amdgcn_permlane16_swap(__float_as_uint(a), __float_as_uint(b), false, false); a = __uint_as_float(r[0]); b = __uint_as_float(r[1]); }
DI void swap32(float& a, float& b) { auto r = __builtin_amdgcn_permlane32_swap(__float_as_uint(a), __float_as_uint(b), false, false); a = __uint_as_float(r[0]); b = __uint_as_float(r[1]); }
template <int OP> DI float op2(float a, float b) { return OP == 0 ? max2f(a, b) : OP == 1 ? min2f(a, b) : a + b; }
template <int OP> DI float rscat(float a, float b, float c, float d) {
  swap16(a, b); swap16(c, d);
  float x = op2<OP>(a, b), y = op2<OP>(c, d);
  swap32(x, y);
  return op2<OP>(x, y);
}
DI void agath(float r, float (&o)[4]) {
  float x = r, y = r;
  swap16(x, y);
  float x2 = x, y2 = y;
  swap32(x, x2); swap32(y, y2);
  o[0] = x; o[2] = x2; o[1] = y; o[3] = y2;
}
#define A_XW 6
#define A_GD 3

DI void lds_wait_ge(unsigned addr, unsigned n) {
  unsigned v, a, sv;
  asm volatile("v_mov_b32 %2, %3\nLW%=:\n\tds_read_b32 %0, %2\n\ts_waitcnt lgkmcnt(0)\n\tv_readfirstlane_b32 %1, %0\n\ts_cmp_ge_u32 %1, %4\n\ts_cbranch_scc1 LD%=\n\ts_sleep 1\n\ts_branch LW%=\nLD%=:"
               : "=&v"(v), "=&s"(sv), "=&v"(a) : "s"(addr), "i"(n) : "memory", "scc");
}
DI void st_wt(void* p, u4 v) { asm volatile("global_store_dwordx4 %0, %1, off sc1\n\ts_nop 1" :: "v"(p), "v"(v) : "memory"); }
DI void st_wt(void* p, unsigned v) { asm volatile("global_store_dword %0, %1, off sc1\n\ts_nop 1" :: "v"(p), "v"(v) : "memory"); }
DI void st_wt(void* p, u2 v) { asm volatile("global_store_dwordx2 %0, %1, off sc1\n\ts_nop 1" :: "v"(p), "v"(v) : "memory"); }
DI void tr88(const u4 (&r)[8], u4 (&o)[8]) {
#pragma unroll
  for (int n = 0; n < 8; ++n)
#pragma unroll
    for (int j = 0; j < 4; ++j) o[n][j] = __builtin_amdgcn_perm(r[2 * j + 1][n >> 1], r[2 * j][n >> 1], (n & 1) ? 0x07060302u : 0x05040100u);
}
DI float sum8(u4 v) {
  f2 a = unpk(v[0]) + unpk(v[1]), b = unpk(v[2]) + unpk(v[3]);
  a += b;
  return a[0] + a[1];
}
#define PFS 65
#define WAVE_SYNC() asm volatile("s_waitcnt lgkmcnt(0)" ::: "memory")
#define PU_N 648
DI u4 ldnt(const void* p) { return __builtin_nontemporal_load((const u4*)p); }
template <int K> DI void prep_load(const bf16* wsrc, int v, int lane, u4 (&r)[8]) {
  const int oc = lane & 7, kb = lane >> 3, g = kb & 3;
  if (K == 1) {
    const bf16* src = wsrc + (v % 9) * 64 + oc * 8;
#pragma unroll
    for (int e = 0; e < 8; ++e) r[e] = ldnt(src + (size_t)wo_k((v / 9) * 2 + (kb >> 2), g, e) * D_);
  } else if (K == 0) {
    const bf16* src = wsrc + (size_t)((v % 9) * 64 + kb * 8) * F_ + (v / 9) * 64 + oc * 8;
#pragma unroll
    for (int e = 0; e < 8; ++e) r[e] = ldnt(src + e * F_);
  } else {
    const bf16* src = wsrc + (size_t)(64 * (v / 9) + 32 * (kb >> 2) + 4 * g) * D_ + (v % 9) * 64 + oc * 8;
#pragma unroll
    for (int e = 0; e < 8; ++e) r[e] = ldnt(src + (e < 4 ? e : 12 + e) * D_);
  }
}
template <int K> DI void prep_stage(int lane, const u4 (&r)[8], u4 (&o)[8], AS3 u4* wstg) {
  const int oc = lane & 7, kb = lane >> 3, g = kb & 3;
  tr88(r, o);
  if (K < 2) {
    AS3 u4* dst = wstg + ((kb >> 2) * 4 + (oc >> 1)) * PFS + g * 16 + (oc & 1) * 8;
#pragma unroll
    for (int n = 0; n < 8; ++n) dst[n] = o[n];
  } else {
    AS3 u4* dst = wstg + ((kb >> 2) * 4 + (oc >> 2) * 2) * PFS + g * 16 + (oc & 3) * 4;
#pragma unroll
    for (int n = 0; n < 8; ++n) dst[(n >> 2) * PFS + (n & 3)] = o[n];
  }
  WAVE_SYNC();
#pragma unroll
  for (int f = 0; f < 8; ++f) o[f] = wstg[f * PFS + lane];
  asm volatile("" : "+v"(o[0]), "+v"(o[1]), "+v"(o[2]), "+v"(o[3]), "+v"(o[4]), "+v"(o[5]), "+v"(o[6]), "+v"(o[7]));
}
template <int K> DI float prep_store(u4* dstb, int v, int lane, const u4 (&o)[8]) {
  const int b = v / 9, w = v % 9;
  float cs = 0.f;
  if (K == 1) {
#pragma unroll
    for (int f = 0; f < 8; ++f) st_wt(dstb + ((size_t)(w * 4 + (f & 3)) * 18 + b * 2 + (f >> 2)) * 64 + lane, o[f]);
  } else if (K == 0) {
#pragma unroll
    for (int f = 0; f < 8; ++f) {
      const int T = b * 4 + (f & 3);
      st_wt(dstb + ((size_t)(T / 3) * 108 + (w * 2 + (f >> 2)) * 3 + T % 3) * 64 + lane, o[f]);
    }
    cs = rscat<2>(sum8(o[0]) + sum8(o[4]), sum8(o[1]) + sum8(o[5]), sum8(o[2]) + sum8(o[6]), sum8(o[3]) + sum8(o[7]));
  } else {
#pragma unroll
    for (int f = 0; f < 8; ++f) {
      const int T = w * 4 + (f & 3), ksg = b * 2 + (f >> 2);
      st_wt(dstb + ((size_t)((ksg / 18) * 12 + T / 3) * 108 + 54 + (ksg % 18) * 3 + T % 3) * 64 + lane, o[f]);
    }
  }
  return cs;
}
DI void phi4(const s8 (&wp)[5], const u4 (&m)[2], const u2 (&t)[2], u4 (&om)[4], u2& ot) {
  const f4 z4 = {0, 0, 0, 0};
  const float L2E = 1.4426950408889634f;
  f4 P[4], Pt;
#pragma unroll
  for (int b = 0; b < 4; ++b) P[b] = mfma32(wp[b & 1], mk8(m[b >> 1][2 * (b & 1)], m[b >> 1][2 * (b & 1) + 1], t[b >> 1][0], t[b >> 1][1]), z4);
  Pt = mfma32(wp[2], __builtin_bit_cast(s8, m[0]), z4);
  Pt = mfma32(wp[3], __builtin_bit_cast(s8, m[1]), Pt);
  Pt = mfma32(wp[4], mk8(t[0][0], t[0][1], t[1][0], t[1][1]), Pt);
  float hi[4], lo[4];
#pragma unroll
  for (int b = 0; b < 4; ++b) { hi[b] = max2f(max3f(P[b][0], P[b][1], P[b][2]), P[b][3]); lo[b] = min2f(min3f(P[b][0], P[b][1], P[b][2]), P[b][3]); }
  const float mxs = max3f(rscat<0>(hi[0], hi[1], hi[2], hi[3]), Pt[0], Pt[1]) * -L2E;
  const float mns = min3f(rscat<1>(lo[0], lo[1], lo[2], lo[3]), Pt[0], Pt[1]) * L2E;
  float mx[4], mn[4];
  agath(mxs, mx); agath(mns, mn);
  float ep[4][4], en[4][4], sp[4], sn[4];
#pragma unroll
  for (int b = 0; b < 4; ++b) {
#pragma unroll
    for (int r = 0; r < 4; ++r) { ep[b][r] = __builtin_amdgcn_exp2f(__builtin_fmaf(P[b][r], L2E, mx[b])); en[b][r] = __builtin_amdgcn_exp2f(__builtin_fmaf(P[b][r], -L2E, mn[b])); }
    const f2 s1 = (f2){ep[b][0], ep[b][1]} + (f2){ep[b][2], ep[b][3]}, s2 = (f2){en[b][0], en[b][1]} + (f2){en[b][2], en[b][3]};
    sp[b] = s1[0] + s1[1]; sn[b] = s2[0] + s2[1];
  }
  float tp[2], tn[2];
#pragma unroll
  for (int c = 0; c < 2; ++c) { tp[c] = __builtin_amdgcn_exp2f(__builtin_fmaf(Pt[c], L2E, mxs)); tn[c] = __builtin_amdgcn_exp2f(__builtin_fmaf(Pt[c], -L2E, mns)); }
  const float ips = __builtin_amdgcn_rcpf(rscat<2>(sp[0], sp[1], sp[2], sp[3]) + (tp[0] + tp[1]));
  const float ins = __builtin_amdgcn_rcpf(rscat<2>(sn[0], sn[1], sn[2], sn[3]) + (tn[0] + tn[1]));
  float ip[4], in[4];
  agath(ips, ip); agath(ins, in);
#pragma unroll
  for (int b = 0; b < 4; ++b) {
    om[b][0] = cvtpk((f2){ep[b][0], ep[b][1]} * ip[b]); om[b][1] = cvtpk((f2){ep[b][2], ep[b][3]} * ip[b]);
    om[b][2] = cvtpk((f2){en[b][0], en[b][1]} * in[b]); om[b][3] = cvtpk((f2){en[b][2], en[b][3]} * in[b]);
  }
  ot = (u2){cvtpk((f2){tp[0], tp[1]} * ips), cvtpk((f2){tn[0], tn[1]} * ins)};
}
#define A_PRIO 2
#define A_HO (64 * 1024)
#define KVQ 3072
#define A_PG 1
#define A_PS 4
#define A_PD 8
#define F_M 20
#define F_G 4
#define F_N 4
#define X_W 2
#define H_SL 8
#define H_DLY 4
#define A_PP 3
DI float smul(float a, float b) { float r = a * b; asm("" : "+v"(r)); return r; }
typedef _Float16 h2 __attribute__((ext_vector_type(2)));
DI unsigned cvtph(f2 v) { return __builtin_bit_cast(unsigned, __builtin_convertvector(v, h2)); }
DI f2 unph(unsigned u) { return __builtin_convertvector(__builtin_bit_cast(h2, u), f2); }
DI void mfma_z(f4& c, s8 a, s8 b) { asm volatile("v_mfma_f32_16x16x32_bf16 %0, %1, %2, 0" : "=v"(c) : "v"(a), "v"(b)); }
DI s8 cv8(f4 a, f4 b) { return mk8(cvtpk((f2){a[0], a[1]}), cvtpk((f2){a[2], a[3]}), cvtpk((f2){b[0], b[1]}), cvtpk((f2){b[2], b[3]})); }
DI float bperm(float v, int a4) { return __int_as_float(__builtin_amdgcn_ds_bpermute(a4, __float_as_int(v))); }
DI void glds16s(const void* g, unsigned l) { asm volatile("s_mov_b32 m0, %1\n\ts_nop 0\n\tglobal_load_lds_dwordx4 %0, off sc1" :: "v"(g), "s"(l) : "memory"); }
DI void lds_add1(unsigned addr) { unsigned a, o; asm volatile("v_mov_b32 %0, %2\n\tv_mov_b32 %1, 1\n\tds_add_u32 %0, %1" : "=&v"(a), "=&v"(o) : "s"(addr) : "memory"); }
DI void lds_set1(unsigned addr) { unsigned a, o; asm volatile("v_mov_b32 %0, %2\n\tv_mov_b32 %1, 1\n\tds_write_b32 %0, %1" : "=&v"(a), "=&v"(o) : "s"(addr) : "memory"); }
DI unsigned lds_arrive(unsigned addr, int lane) {
  unsigned old, a;
  UNDEF(old);
  if (lane == 0) asm volatile("v_mov_b32 %1, %2\n\tv_mov_b32 %0, 1\n\tds_add_rtn_u32 %0, %1, %0\n\ts_waitcnt lgkmcnt(0)" : "=&v"(old), "=&v"(a) : "s"(addr) : "memory");
  return __builtin_amdgcn_readfirstlane(old);
}
DI void flag_set(unsigned* p) { unsigned o; asm volatile("v_mov_b32 %0, 1\n\tglobal_store_dword %1, %0, off sc1\n\ts_nop 1" : "=&v"(o) : "v"(p) : "memory"); }
DI void flag_add(unsigned* p) { unsigned o; asm volatile("v_mov_b32 %0, 1\n\tglobal_atomic_add %1, %0, off sc1\n\ts_nop 1" : "=&v"(o) : "v"(p) : "memory"); }
DI void flag_poll(const unsigned* p, unsigned want) {
#pragma unroll 1
  for (int it = 0; it < 100000; ++it) {
    unsigned v;
    asm volatile("global_load_dword %0, %1, off sc1\n\ts_waitcnt vmcnt(0)" : "=v"(v) : "v"(p) : "memory");
    if (__builtin_amdgcn_ballot_w64(v == want) == ~0ull) break;
    __builtin_amdgcn_s_sleep(1);
  }
}
typedef __amdgpu_buffer_rsrc_t rsrc_t;
DI u4 bld4(rsrc_t r, unsigned vo, unsigned so) { return __builtin_amdgcn_raw_buffer_load_b128(r, vo, so, 16); }
DI u2 bld2(rsrc_t r, unsigned vo, unsigned so) { return __builtin_amdgcn_raw_buffer_load_b64(r, vo, so, 16); }
#define X_AUX 16
DI s8 bld8x(rsrc_t r, unsigned vo, unsigned so) { return __builtin_bit_cast(s8, __builtin_amdgcn_raw_buffer_load_b128(r, vo, so, X_AUX)); }
DI s8 bld8(rsrc_t r, unsigned vo, unsigned so) { return __builtin_bit_cast(s8, bld4(r, vo, so)); }
DI f4 bldf(rsrc_t r, unsigned vo, unsigned so) { return __builtin_bit_cast(f4, bld4(r, vo, so)); }

#define XBY 2359296u
#define O_X(L) ((L) * XBY)
#define O_PART(L) ((4 + 8 * (L)) * XBY)
#define O_WM (20 * XBY)
#define O_WQKV (O_WM + 10616832u)
#define O_WO (O_WQKV + 2066432u)
#define O_WPHI (O_WO + 663552u)
#define O_CB1 (O_WPHI + 131072u)
#define O_CSP (O_CB1 + 40960u)
#define O_STA0 (O_CSP + 65536u)
#define O_HB (O_STA0 + 32768u)
#define O_FP (O_HB + 16384u)
#define O_FP2 (O_FP + 16384u)
#define O_FM (O_FP2 + 16384u)
#define O_FR (O_FM + 49152u)
#define O_ST1(L) (O_FR + 16384u + (L) * 65536u)
#define O_STA1 O_ST1(2)
#define SLABBY 786432u
#define SOBY (SLABBY + XBY)
#define O_SLAB(L) (O_STA1 + 32768u + (L) * SOBY)
#define OBY (SOBY + XBY)
#define O_O(L) (O_SLAB(0) + SLABBY + (L) * OBY)
#define O_X1(L) (O_SLAB(1) + SLABBY + (L) * 2 * XBY)
#define O_END (O_X1(1) + XBY)
DI unsigned umax3(unsigned a, unsigned b, unsigned c) { return max(max(a, b), c); }
DI unsigned umax4(unsigned m, u4 v) { return umax3(umax3(m, v[0], v[1]), v[2], v[3]); }
DI bool any_ones(unsigned m) { return __builtin_amdgcn_ballot_w64(m == ~0u) != 0ull; }
#define RETRY 30000
#define SL1 1
#define SL4 2
#define F_B 0
#define W3F 0
#define MDE 0
#define RR_C 9
#define IPF 11
#define A_WP 12
#define SL5 2
#define R_DLY 0
#define A_ST 14
#define R_PACE 8
#define PR_A 13
#define PR_B 4
#define PR_E 0
#define SL2 1
template <int N, int SL, class LD, class BAD> DI void poll_gen(u4 (&p)[N], LD ld, BAD bad) {
  unsigned pend = 0;
#pragma unroll
  for (int s = 0; s < N; ++s) p[s] = ld(s);
#pragma unroll
  for (int s = 0; s < N; ++s) if (bad(p[s])) pend |= 1u << s;
#pragma unroll 1
  for (int it = 0; pend != 0 && it < RETRY; ++it) {
    asm volatile("s_sleep %0" :: "i"(SL) : "memory");
#pragma unroll
    for (int s = 0; s < N; ++s) if ((pend >> s) & 1) p[s] = ld(s);
#pragma unroll
    for (int s = 0; s < N; ++s) if ((pend >> s) & 1) { if (!bad(p[s])) pend &= ~(1u << s); }
  }
}
template <int N, int SL, class LD> DI void poll_load(u4 (&p)[N], LD ld) {
  poll_gen<N, SL>(p, ld, [](u4 v) { return any_ones(umax4(0u, v)); });
}
DI float dot2(unsigned a, unsigned b, float c) { return __builtin_amdgcn_fdot2_f32_bf16(__builtin_bit_cast(bf2, a), __builtin_bit_cast(bf2, b), c, false); }

#define QORD 1
#ifndef M_PD
#define M_PD 4
#endif
#define M_NS 2
#define M_CB (2 * 73728)
#define R_RED (M_CB + 5120)
#define P_STG (128 * 1024)
#define A_CS (P_STG + 25088)
#define M_FL (A_CS + 512)
#define BO (104 * 1024)
#define BRED (BO + 36 * 1024)
#define BM_PF 12
#define BM_S0 3
#define BM_A 4
#define BM_B 8
#define DLY 4
#define DLY2 4
#define SL3 1
#define BM_PF2 9
#define B_E 18
#define P_W1 1
#define P_W2 2
#define B_LM 1
#define RA_P1 18
enum { W_HO = 0, W_KV = 4, W_ADR, W_GOA, W_WL, W_GOB, W_OR, W_BRED, W_BLD, W_BDR, W_GOM, W_XL, W_H0, W_H1, W_H2, W_MDN, W_GOR, W_GOR1, W_GOR2, W_RRED, W_PDR, W_GOP, W_PD2, W_SL, W_GOQ, W_PRE, W_GO0 };

__global__ __launch_bounds__(768, 3) void kF(char* ws0, const bf16* p_x0, const bf16* p_wqkv, const bf16* p_wphi, const bf16* p_wo, const bf16* p_w1, const bf16* p_b1,
                                             const bf16* p_w2, const bf16* __restrict__ b2, bf16* out) {
  __shared__ __attribute__((aligned(16))) char smem[M_FL + 256 + STM_BYTES];
  const f4 z4 = {0, 0, 0, 0};
  if (threadIdx.x >= 704) ((AS3 unsigned*)(smem + M_FL))[threadIdx.x - 704] = 0u;
  const unsigned gop = lds_addr(smem) + M_FL + W_GOP * 4;
  const int w0 = __builtin_amdgcn_readfirstlane(threadIdx.x >> 6);
  {
    const int w = w0, bid = blockIdx.x, L = 0, xcd = bid & 7, hp = (xcd >> 2) * 8 + (bid >> 5);
    char* ws = ws0;
    int lane, fi, fg;
    unsigned l16;
    RELANE();
    const rsrc_t rs = __builtin_amdgcn_make_buffer_rsrc(ws, 0, O_END, 0x00020000);
    auto FW = [&](int i) { return lds_addr(smem) + M_FL + i * 4; };
    STM(0);
    if (IPF && w == 0) {
      unsigned long long pc; int len; unsigned d;
      asm volatile("s_getpc_b64 %0\nLPC0:\n\ts_mov_b32 %1, LKEND-LPC0" : "=s"(pc), "=s"(len));
      const int off = ((bid >> 3) % IPF) * 8192 + lane * 128;
      if (off < len) asm volatile("global_load_dword %0, %1, %2\n\ts_waitcnt vmcnt(0)" : "=v"(d) : "v"(off), "s"(pc) : "memory");
    }
    if (w >= 8) {
      const int s1 = bid * 3 + (w - 9);
      {
        if (w == 8) {
          const unsigned long long t0 = __builtin_amdgcn_s_memrealtime();
          PST(0);
          BARRIER();
          char* hb = ws + O_HB + (lane & 7) * 2048 + bid * 8;
          if (lane < 8) st_wt(hb, __builtin_bit_cast(u2, __builtin_amdgcn_s_memrealtime()));
          STM(2);
          asm volatile("s_sleep %0" :: "i"(H_DLY) : "memory");
#pragma unroll 1
          for (int it = 0; it < RETRY; ++it) {
            const u4 h0 = bld4(rs, l16, O_HB + xcd * 2048), h1 = bld4(rs, l16, O_HB + xcd * 2048 + 1024);
            auto d = [&](unsigned lo, unsigned hi) { return (((unsigned long long)hi << 32 | lo) - t0) >> 24; };
            const bool ok = (d(h0[0], h0[1]) | d(h0[2], h0[3]) | d(h1[0], h1[1]) | d(h1[2], h1[3])) == 0ull;
            if (__builtin_amdgcn_ballot_w64(ok) == ~0ull) break;
            if ((it & 7) == 7 && lane < 8) st_wt(hb, __builtin_bit_cast(u2, __builtin_amdgcn_s_memrealtime()));
            __builtin_amdgcn_s_sleep(1);
          }
          if (lane == 0) lds_set1(FW(W_GO0));
          if (lane < 8) st_wt(hb, __builtin_bit_cast(u2, __builtin_amdgcn_s_memrealtime()));
          STM(3);
          f4 cp[9];
#pragma unroll
          for (int g = 0; g < 9; ++g) {
            if (g == 8) WAIT_V(0);
            BARRIER();
            if (g == 0) {
#pragma unroll
              for (int e = 0; e < 9; ++e) cp[e] = bldf(rs, min(lane, 27) * 16, O_CSP + e * 7168 + hp * 448);
              SB();
            }
            if (g == 3) {
              const f4 c = ((cp[0] + cp[1]) + (cp[2] + cp[3])) + ((cp[4] + cp[5]) + (cp[6] + cp[7])) + cp[8];
              if (lane < 28) *(AS3 f4*)(smem + A_CS + l16) = c;
            }
            if (g >= F_G && g < F_G + 12 / F_N) {
#pragma unroll
              for (int i = (g - F_G) * F_N; i < (g - F_G + 1) * F_N; ++i) st_wt(ws + O_SLAB(1) + bid * 12288 + i * 1024 + l16, (u4){~0u, ~0u, ~0u, ~0u});
            }
            SB();
          }
        } else {
          const bool one = s1 < PU_N;
          u4 r[8], o[8];
          if (A_PG < 0) { asm volatile("s_sleep %0" :: "i"(A_PD) : "memory"); if (one) prep_load<0>(p_w1, s1, lane, r); SB(); }
          BARRIER();
          SB();
          if (A_PG < 0) { if (one) prep_stage<0>(lane, r, o, (AS3 u4*)(smem + P_STG) + (w - 9) * 8 * PFS); SB(); }
          __builtin_amdgcn_s_setprio(A_PP);
#pragma unroll
          for (int g = 0; g < 9; ++g) {
            BAR0();
            if (g == A_PG) { if (one) prep_load<0>(p_w1, s1, lane, r); SB(); }
            if (g == A_PS) {
              if (one) {
                if (A_PG >= 0) prep_stage<0>(lane, r, o, (AS3 u4*)(smem + P_STG) + (w - 9) * 8 * PFS);
                STM(1);
                const float cs1 = prep_store<0>((u4*)(ws + O_WM), s1, lane, o);
                unsafeAtomicAdd((float*)(ws + O_CB1) + ((s1 / 9) * 16 + (lane >> 2)) * 8 + (lane & 3), cs1);
              }
              SB();
            }
          }
        }
      }
    } else if (w >= 4) {
      const int lw = w - 4;
      {
        AS3 u4* wstg = (AS3 u4*)smem + lw * 8 * PFS;
        if (w == 4) {
          if (bid < 243) {
          const int cb = bid / 9, kq = bid % 9;
          u4 r[8], o[8];
          const int oc = lane & 7, kb = lane >> 3;
          const bf16* src = p_wqkv + (size_t)(kq * 64 + kb * 8) * (3 * D_) + cb * 64 + oc * 8;
#pragma unroll
          for (int e = 0; e < 8; ++e) r[e] = ldnt(src + e * (3 * D_));
          SB();
          PST(0);
          const int c = cb * 64 + lane, th = c / 576, cc = c % 576, hd = cc / 18, i = cc % 18, j = th * 2 + (hd & 1);
          u4* fd = (u4*)(ws + O_WQKV) + (size_t)(hd >> 1) * 126 * 64 + (kq * 2 * 7) * 64 + (i < 16 ? j * 64 + i : 6 * 64 + 2 * j + i - 16);
          float* q = (float*)(ws + O_CSP) + kq * 1792 + (hd >> 1) * 112 + (i < 16 ? j * 16 + i : 96 + 2 * j + i - 16);
          if (th == 2 && i >= 16) {
#pragma unroll
            for (int f = 0; f < 8; ++f) st_wt(fd + (f >> 2) * 7 * 64 + (f & 3) * 16 + 4, (u4){0u, 0u, 0u, 0u});
            st_wt(q + 4, 0u);
          }
          PST(2);
          asm volatile("" :: "v"(r[0]), "v"(r[7]));
          PST(3);
          tr88(r, o);
          AS3 u4* dst = wstg + kb * PFS + oc * 8;
#pragma unroll
          for (int n = 0; n < 8; ++n) dst[n] = o[n];
          WAVE_SYNC();
#pragma unroll
          for (int f = 0; f < 8; ++f) o[f] = wstg[f * PFS + lane];
#pragma unroll
          for (int f = 0; f < 8; ++f) st_wt(fd + (f >> 2) * 7 * 64 + (f & 3) * 16, o[f]);
          const float cs = ((sum8(o[0]) + sum8(o[1])) + (sum8(o[2]) + sum8(o[3]))) + ((sum8(o[4]) + sum8(o[5])) + (sum8(o[6]) + sum8(o[7])));
          st_wt(q, __float_as_uint(cs));
          PST(4);
          }
        } else if (w == 5) {
          const int ce = bid * 9 + min(lane, 8);
          const char* src = (const char*)p_x0 + bid * 9216 + l16;
#pragma unroll
          for (int q = 0; q < 9; ++q) glds16a(src + q * 1024, lds_addr(smem) + 40960 + q * 1024);
          const u2 bv = *(const u2*)(p_b1 + (ce >> 1) * 4);
          SB();
          PST(5);
          asm volatile("s_waitcnt vmcnt(1)" ::: "memory");
          PST(6);
          const int tk = lane & 7, g = (lane >> 3) & 3, kh = lane >> 5;
          u4 v[9];
#pragma unroll
          for (int q = 0; q < 9; ++q) v[q] = *(const AS3 u4*)(smem + 40960 + tk * 1152 + kh * 64 + g * 16 + q * 128);
          char* xd = ws + O_X(0) + ((bid >> 1) * 18 + kh) * 1024 + (g * 16 + (bid & 1) * 8 + tk) * 16;
#pragma unroll
          for (int q = 0; q < 9; ++q) st_wt(xd + q * 2048, v[q]);
          SB();
          auto red8 = [&](float x) { return red_sum(x + __int_as_float(__builtin_amdgcn_update_dpp(0, __float_as_int(x), 0x128, 0xf, 0xf, false))); };
          float sm = 0.f;
#pragma unroll
          for (int q = 0; q < 9; ++q) sm += sum8(v[q]);
          const float m = red8(sm) * (1.0f / 576.0f);
          f2 m2 = {0.f, 0.f};
#pragma unroll
          for (int q = 0; q < 9; ++q)
#pragma unroll
            for (int c = 0; c < 4; ++c) { f2 d = unpk(v[q][c]) - m; m2 += d * d; }
          const float q2 = red8(m2[0] + m2[1]);
          if (lane < 8) st_wt(ws + O_STA0 + (bid * 8 + lane) * 16, __builtin_bit_cast(u4, (f4){m, q2, m, 0.f}));
          const unsigned mk = (ce & 1) ? ~0u : 0u;
          if (lane < 9) st_wt(ws + O_CB1 + ce * 16, (u4){(bv[0] << 16) & mk, bv[0] & 0xffff0000u & mk, (bv[1] << 16) & mk, bv[1] & 0xffff0000u & mk});
          PST(7);
        } else if (w == 6) {
          if (bid < 81) {
            u4 r[8], o[8];
            prep_load<1>(p_wo, bid, lane, r);
            SB();
            PST(10);
            prep_stage<1>(lane, r, o, wstg);
            prep_store<1>((u4*)(ws + O_WO), bid, lane, o);
          } else if (bid < 209) {
            const int wu = bid - 81, f = wu & 7, hq = wu >> 3;
            unsigned short val[8];
            unsigned okm = 0;
#pragma unroll
            for (int e = 0; e < 8; ++e) {
              int bb = 0, d = 0, ft = 0; bool ok = false;
              const int rho = 4 * fg + (e & 3);
              if (f < 4) {
                bb = f; ft = fi;
                if (e < 4) { d = rho; ok = true; }
                else { d = 16 + (rho & 1); ok = (rho >> 1) == bb; }
              } else {
                const int c = fi & 3, sl = fi >> 2;
                bb = sl; ft = 16 + (c & 1);
                if (f == 4) { d = rho; ok = bb == (e >> 2); }
                else if (f == 5) { d = rho; ok = bb == 2 + (e >> 2); }
                else { d = 16 + (rho & 1); ok = (e >> 2) == (sl >> 1) && (rho >> 1) == (f - 6) * 2 + (sl & 1); }
                ok = ok && c < 2;
              }
              val[e] = __builtin_bit_cast(unsigned short, p_wphi[((2 * hq + (bb & 1)) * DH_ + d) * DH_ + ft]);
              okm |= ok ? 1u << e : 0u;
            }
            SB();
            PST(11);
            u4 ov;
#pragma unroll
            for (int e = 0; e < 4; ++e) ov[e] = ((okm >> (2 * e)) & 1 ? (unsigned)val[2 * e] : 0u) | ((okm >> (2 * e + 1)) & 1 ? (unsigned)val[2 * e + 1] << 16 : 0u);
            st_wt(ws + O_WPHI + (wu * 64 + lane) * 16, ov);
          }
        } else {
          const unsigned zo = bid * 1024 + l16, v = zo < O_ST1(0) - O_FP ? 0u : 0xbf800000u;
          st_wt(ws + O_FP + zo, (u4){v, v, v, v});
#pragma unroll
          for (int i = 0; i < 12; ++i) st_wt(ws + O_SLAB(0) + bid * 12288 + i * 1024 + l16, (u4){~0u, ~0u, ~0u, ~0u});
          PST(8);
        }
        WAIT_V(0);
        STM(6);
        BAR0();
        lds_wait_ge(FW(W_GO0), 1);
        STM(7);
      }
    }
  }
#pragma unroll 1
  for (int L = 0; L < 2; ++L) {
    int w = w0, bid = blockIdx.x;
    AS1 char* wsg = (AS1 char*)ws0;
    asm volatile("" : "+s"(w), "+s"(bid), "+s"(wsg));
    char* ws = (char*)wsg;
    int lane, fi, fg;
    unsigned l16;
    RELANE();
    const int xcd = bid & 7, li = bid >> 3;
    const rsrc_t rs = __builtin_amdgcn_make_buffer_rsrc(ws, 0, O_END, 0x00020000);
    const int art = (xcd & 3) * 4 + (li & 3), hp = (xcd >> 2) * 8 + (li >> 2);
    const int rtb = xcd * 8 + (li >> 2), ct = li & 3;
    const int slice = (xcd % (8 / M_NS)) * M_NS + li % M_NS, rt = (xcd / (8 / M_NS)) * (32 / M_NS) + li / M_NS;
    const unsigned fla = lds_addr(smem) + M_FL + L * 128;
    auto FW = [&](int i) { return fla + i * 4; };
    if (L == 1) STM(0);
    s8 wf[18];
    const unsigned wobase = O_WO + (ct * 9 + w) * 18 * 1024;
    auto wfl = [&](int s) { return bld8(rs, l16, wobase + s * 1024); };
    const unsigned wbo = O_WM + (slice * 12 + w) * 108 * 1024;
    auto wld = [&](int u, int t) { return bld8(rs, l16, wbo + (u * 3 + t) * 1024); };
    s8 wb[M_PD][3];
    const int tt0 = art * 8 + (w & 3) * 2;
    auto slab_sum = [&]() {
      const int j = min(lane & 31, 23);
      const unsigned vo = (lane >> 5) * 8 * 3072 + j * 16, so = O_SLAB(0) + L * SOBY + hp * 16 * 3072 + w * 384;
      u4 p[8];
      poll_load<8, SL1>(p, [&](int s) { return bld4(rs, vo, so + s * 3072); });
      unsigned one_lo, one_hi;
      asm volatile("v_mov_b32 %0, 0x3f80\n\tv_mov_b32 %1, 0x3f800000" : "=v"(one_lo), "=v"(one_hi));
      u4 o;
#pragma unroll
      for (int c = 0; c < 4; ++c) {
        f2 a = unpk(p[0][c]);
#pragma unroll
        for (int s = 1; s < 8; ++s) { a[0] = dot2(p[s][c], one_lo, a[0]); a[1] = dot2(p[s][c], one_hi, a[1]); }
        float x = a[0], y = a[1];
        swap32(x, y);
        a[0] = x + y;
        x = a[0]; y = a[0];
        swap32(x, y);
        o[c] = cvtpk((f2){x, y});
      }
      if (lane < 24) *(AS3 u4*)(smem + w * 384 + lane * 16) = o;
      asm volatile("s_waitcnt lgkmcnt(0)" ::: "memory");
      if (lane == 0) lds_add1(FW(W_GOB));
    };
    if (w >= 8) {
      const int s1 = bid * 3 + (w - 9);
      const bool one = w > 8 && s1 < PU_N && L == 0;
      u4 r[8], o[8];
#pragma unroll
      for (int e = 0; e < 8; ++e) { UNDEF(r[e]); UNDEF(o[e]); }
      if (L == 0) {
        AS3 u4* pst = (AS3 u4*)(smem + 76 * 1024) + (w - 9) * 8 * PFS;
        __builtin_amdgcn_s_setprio(0);
        if (one) {
          prep_load<2>(p_w2, s1, lane, r);
          SB();
          WAIT_V(8);
        }
        if (w > 8) {
          if (lds_arrive(FW(W_PDR), lane) == 2 && lane < 8) flag_add((unsigned*)(ws + O_FP + (lane * 16 + hp) * 128));
          STM(2);
          if (one) prep_stage<2>(lane, r, o, pst);
          STM(3);
          if (w == 11) {
            flag_poll((const unsigned*)(ws + O_FP + (xcd * 16 + fi) * 128), 16u);
            if (lane == 0) lds_set1(gop);
          }
          STM(4);
        }
      } else lds_wait_ge(FW(W_HO), 1);
      if (w == 8) {
#pragma unroll
        for (int s = 0; s < B_E; ++s) { wf[s] = wfl(s); SB(); }
      } else {
#pragma unroll
        for (int s = 0; s < B_E; ++s) UNDEF(wf[s]);
      }
      lds_wait_ge(gop, 1);
#pragma unroll
      for (int i = 0; i < 2; ++i) { const int p = (w - 9) * 2 + i; if (w > 8 && p < 5 && L == 0) glds16s(ws + O_CB1 + slice * 4608 + p * 1024 + l16, lds_addr(smem) + M_CB + p * 1024); }
#pragma unroll
      for (int q = 0; q < 3 * M_PD; ++q) { wb[q / 3][q % 3] = wld(q / 3, q % 3); SB(); }
      if (one) {
        if (P_W2 == 1) lds_wait_ge(FW(W_BLD), 8);
        if (P_W2 == 2) lds_wait_ge(FW(W_OR), 8);
        if (P_W2 == 3) lds_wait_ge(FW(W_BDR), 9);
        prep_store<2>((u4*)(ws + O_WM), s1, lane, o);
      }
      STM(5);
    } else if (w >= 4) {
      const int lw = w - 4;
      s8 wp[5];
      if (L == 0) {
        const char* src = ws + O_WQKV + (size_t)hp * 126 * 1024 + lw * 1024 + l16;
        const unsigned dst = lds_addr(smem) + lw * 1024;
        auto cum = [](int g) { return g >= 8 ? 32 : (14 * (g + 1) + 3) / 4; };
#pragma unroll
        for (int i = 0; i < cum(A_GD - 1); ++i) glds16s(src + i * 4096, dst + i * 4096);
#pragma unroll
        for (int g = 0; g < 9; ++g) {
          asm volatile("s_waitcnt vmcnt(%0)\n\ts_barrier" :: "i"(cum(g + A_GD - 1) - cum(g)) : "memory");
#pragma unroll
          for (int i = cum(g + A_GD - 1); i < cum(g + A_GD); ++i) glds16s(src + i * 4096, dst + i * 4096);
        }
      }
#pragma unroll
      for (int f = 0; f < 5; ++f) wp[f] = bld8(rs, l16, O_WPHI + (hp * 8 + (f < 2 ? f : f + 2)) * 1024);
      SB();
      if (L == 1) lds_wait_ge(FW(W_HO + lw), 1);
#pragma unroll
      for (int s = 0; s < B_E; ++s) { wf[s] = wfl(s); SB(); }
      lds_wait_ge(FW(W_HO + lw), 1);
      STM(1);
      const AS3 u4* ho = (const AS3 u4*)(smem + A_HO + lw * 3072) + lane;
      const u4 m[2] = {ho[0], ho[64]};
      const u4 tq = ho[128];
      const u2 t[2] = {(u2){tq[0], tq[1]}, (u2){tq[2], tq[3]}};
      u4 om[4]; u2 ot;
      phi4(wp, m, t, om, ot);
      STM(2);
      lds_wait_ge(FW(W_SL), 4);
      STM(3);
      slab_sum();
      STM(4);
      lds_wait_ge(FW(W_GOB), 8);
      {
        const AS3 char* kl = (const AS3 char*)smem;
        u2 t3 = *(const AS3 u2*)(kl + 2816 + (fg & 1) * 32 + (fi & 3) * 8);
        const u2 t1 = *(const AS3 u2*)(kl + 2048 + (fg & 1) * 128 + fi * 8);
        const u4 f2a = *(const AS3 u4*)(kl + 2304 + (fg * 8 + (fi & 7)) * 16);
        const s8 f0[2] = {*(const AS3 s8*)(kl + l16), *(const AS3 s8*)(kl + 1024 + l16)};
        const s8 bt = mk8(ot[0], ot[1], 0u, 0u);
        if ((fi >> 2) != fg) t3 = (u2){0u, 0u};
        f4 ov[4], otl = mfma32(mk8(t3[0], t3[1], 0u, 0u), bt, z4);
#pragma unroll
        for (int b = 0; b < 4; ++b) {
          const bool s2 = (fi >> 2) == b;
          otl = mfma32(mk8(s2 ? f2a[0] : 0u, s2 ? f2a[1] : 0u, s2 ? f2a[2] : 0u, s2 ? f2a[3] : 0u), __builtin_bit_cast(s8, om[b]), otl);
        }
#pragma unroll
        for (int b = 0; b < 4; ++b) {
          const bool s1 = fg == b;
          ov[b] = mfma32(mk8(s1 ? t1[0] : 0u, s1 ? t1[1] : 0u, 0u, 0u), bt, z4);
          ov[b] = mfma32(f0[b & 1], __builtin_bit_cast(s8, om[b]), ov[b]);
        }
        const float invs = __builtin_amdgcn_rcpf(otl[2] + 1e-6f);
        float inv[4];
        agath(invs, inv);
        char* op = ws + O_O(0) + (size_t)L * OBY + (art * 4 + lw) * 36864;
        auto po = [&](int b, int r) { return cvtpk((f2){ov[b][2 * r], ov[b][2 * r + 1]} * inv[b]); };
#pragma unroll
        for (int mt = 0; mt < 2; ++mt) st_wt(op + (hp * 2 + mt) * 1024 + l16, (u4){po(2 * mt, 0), po(2 * mt, 1), po(2 * mt + 1, 0), po(2 * mt + 1, 1)});
        st_wt(op + 32768 + hp * 256 + lane * 4, cvtpk((f2){otl[0], otl[1]} * invs));
      }
      STM(5);
#pragma unroll
      for (int q = 0; q < 3 * M_PD; ++q) UNDEF(wb[q / 3][q % 3]);
    } else {
      const unsigned xo_ = L * XBY + tt0 * 18432;
      auto xld = [&](int p, int mt) { return bld8(rs, l16, xo_ + (mt * 18 + p) * 1024); };
      const AS3 s8* wl = (const AS3 s8*)smem + lane;
      const AS3 s8* wl2 = opaque_lds(wl + 4096);
      auto wrd = [&](int piece) { return piece < 64 ? wl[piece * 64] : wl2[(piece - 64) * 64]; };
      f4 acc[7][2], st[2];
      s8 wp[5];
      auto gemm = [&](auto lc) {
      constexpr int LL = decltype(lc)::value;
      s8 xb[18][2];
      if (LL == 0) {
        BAR0(); lds_wait_ge(FW(W_GO0), 1);
#pragma unroll
        for (int p = 0; p < 6; ++p) { xb[p][0] = xld(p, 0); xb[p][1] = xld(p, 1); SB(); }
        __builtin_amdgcn_s_setprio(A_PRIO);
        BAR0();
      } else {
#pragma unroll
        for (int p = 0; p < 6; ++p) { UNDEF(xb[p][0]); UNDEF(xb[p][1]); }
        __builtin_amdgcn_s_setprio(0);
        lds_wait_ge(FW(W_WL), 4);
      }
      s8 wfa[7];
      STM(1);
#pragma unroll
      for (int j = 0; j < 7; ++j) wfa[j] = wl[j * 64];
      SB();
#pragma unroll
      for (int ug = 0; ug < 18; ug += 6) {
      if (LL == 1) {
        u4 p[12];
        if (ug == 6) lds_wait_ge(FW(W_RRED) - 128, 6);
        if (ug == 12) lds_wait_ge(FW(W_RRED) - 128, RR_C);
        if (ug == 6) GST(7);
        if (ug == 12) GST(10);
        poll_load<12, SL4>(p, [&](int i) { return bld4(rs, l16, xo_ + ((i & 1) * 18 + ug + (i >> 1)) * 1024); });
#pragma unroll
        for (int i = 0; i < 12; ++i) xb[ug + (i >> 1)][i & 1] = __builtin_bit_cast(s8, p[i]);
        if (ug == 0) GST(6);
        if (ug == 6) GST(8);
        if (ug == 12) { __builtin_amdgcn_s_setprio(A_PRIO); STM(5); }
        SB();
      }
#pragma unroll
      for (int u = ug; u < ug + 6; ++u) {
        const bool rd = u + 1 < 18;
        if (rd && (u & 1)) { if (LL == 0) BAR0(); }
        if (u == 9 && LL == 1) { lds_wait_ge(FW(W_ADR), 4); GST(9); SB(); }
#pragma unroll
        for (int j = 0; j < 7; ++j) {
          if (u == 0) mfma_z(acc[j][0], wfa[j], xb[u][0]); else mfma_acc(acc[j][0], wfa[j], xb[u][0]);
          SB();
          if (j > 0) { if (rd) wfa[j - 1] = wrd((u + 1) * 7 + j - 1); }
          else if (u > 0) wfa[6] = wrd(u * 7 + 6);
          SB();
          if (u == 0) mfma_z(acc[j][1], wfa[j], xb[u][1]); else mfma_acc(acc[j][1], wfa[j], xb[u][1]);
          SB();
        }
        if (u + 6 < 18) {
          if (LL == 0) { xb[u + 6][0] = xld(u + 6, 0); xb[u + 6][1] = xld(u + 6, 1); } else { UNDEF(xb[u + 6][0]); UNDEF(xb[u + 6][1]); }
          SB();
        }
        if (u == A_ST) {
#pragma unroll
          for (int mt = 0; mt < 2; ++mt) st[mt] = bldf(rs, (lane & 15) * 16, O_STA0 + LL * (O_STA1 - O_STA0) + (tt0 + mt) * 256);
          SB();
        }
        if (u == A_WP) {
#pragma unroll
          for (int f = 0; f < 5; ++f) wp[f] = bld8(rs, l16, O_WPHI + (hp * 8 + (f < 4 ? f + 2 : 7)) * 1024);
          SB();
        }
        if (u == 12 && LL == 1) { if (lane == 0) lds_add1(FW(W_ADR)); SB(); }
      }
      }
      };
      if (L == 0) gemm(IC<0>{}); else gemm(IC<1>{});
      MFMA_FENCE();
      SB();
      RELANE();
      STM(2);
      if (L == 1) lds_wait_ge(FW(W_ADR), 8);
#pragma unroll 1
      for (int it = 0; it < RETRY; ++it) {
        const bool bad = st[0][1] == -1.f || st[0][3] == -1.f || st[1][1] == -1.f || st[1][3] == -1.f;
        if (__builtin_amdgcn_ballot_w64(bad) == 0ull) break;
#pragma unroll
        for (int mt = 0; mt < 2; ++mt) st[mt] = bldf(rs, (lane & 15) * 16, O_STA0 + L * (O_STA1 - O_STA0) + (tt0 + mt) * 256);
      }
      AS3 char* stg = (AS3 char*)smem + w * 10240;
      float rstd[2], nmr[2];
#pragma unroll
      for (int mt = 0; mt < 2; ++mt) { float mean; ln_comb2(st[mt], mean, rstd[mt]); nmr[mt] = -mean * rstd[mt]; }
      f4 cs[7];
#pragma unroll
      for (int j = 0; j < 7; ++j) cs[j] = *(const AS3 f4*)(smem + A_CS + j * 64 + fg * 16);
      auto pkc = [&](int j, int mt, int r) {
        return cvtpk(__builtin_elementwise_fma((f2){acc[j][mt][2 * r], acc[j][mt][2 * r + 1]}, (f2){rstd[mt], rstd[mt]}, (f2){cs[j][2 * r], cs[j][2 * r + 1]} * nmr[mt]));
      };
      u2 t[2];
      {
        AS3 u4* ho = (AS3 u4*)(smem + A_HO + w * 3072) + lane;
#pragma unroll
        for (int mt = 0; mt < 2; ++mt) {
          ho[mt * 64] = (u4){pkc(0, mt, 0), pkc(0, mt, 1), pkc(1, mt, 0), pkc(1, mt, 1)};
          t[mt] = (u2){pkc(6, mt, 0), pkc(6, mt, 1)};
        }
        ho[128] = (u4){t[0][0], t[0][1], t[1][0], t[1][1]};
        asm volatile("s_waitcnt lgkmcnt(0)" ::: "memory");
        if (lane == 0) lds_set1(FW(W_HO + w));
        SB();
      }
      {
        u4 m[2], om[4]; u2 ot;
        unsigned one16;
        asm volatile("v_mov_b32 %0, 0x3f80" : "=v"(one16));
#pragma unroll
        for (int mt = 0; mt < 2; ++mt) m[mt] = (u4){pkc(2, mt, 0), pkc(2, mt, 1), pkc(3, mt, 0), pkc(3, mt, 1)};
#pragma unroll
        for (int mt = 0; mt < 2; ++mt) {
          AS3 char* row = stg + (mt * 16 + fi) * 160;
#pragma unroll
          for (int hh = 0; hh < 2; ++hh) {
            *(AS3 u2*)(row + hh * 5120 + 80 + fg * 8) = (u2){pkc(4 + hh, mt, 0), pkc(4 + hh, mt, 1)};
            if (fg == 2) *(AS3 u2*)(row + hh * 5120 + 112) = (u2){t[mt][hh], one16};
          }
        }
        phi4(wp, m, t, om, ot);
#pragma unroll
        for (int b = 0; b < 4; ++b) *(AS3 u4*)(stg + (b & 1) * 5120 + ((b >> 1) * 16 + fi) * 160 + fg * 16) = om[b];
        *(AS3 u2*)(stg + (fg & 1) * 5120 + ((fg >> 1) * 16 + fi) * 160 + 64) = ot;
      }
      {
        AS3 float* img = (AS3 float*)(smem + 40 * 1024) + w * 1536;
#pragma unroll
        for (int hh = 0; hh < 2; ++hh) {
          const AS3 char* bk = stg + hh * 5120 + (4 * fg + (fi >> 2)) * 160 + (fi & 3) * 8;
          s8 bv[2];
#pragma unroll
          for (int nt = 0; nt < 2; ++nt) bv[nt] = __builtin_shufflevector(trread(bk + 80 + nt * 32), trread(bk + 80 + nt * 32 + 16 * 160), 0, 1, 2, 3, 4, 5, 6, 7);
#pragma unroll
          for (int mt = 0; mt < 3; ++mt) {
            s8 a = __builtin_shufflevector(trread(bk + mt * 32), trread(bk + mt * 32 + 16 * 160), 0, 1, 2, 3, 4, 5, 6, 7);
            f4 k0 = mfma32(a, bv[0], z4), k1 = mfma32(a, bv[1], z4);
            if (mt < 2) {
              *(AS3 f4*)(img + hh * 512 + ((2 * mt + (fg >> 1)) * 16 + fi) * 8 + (fg & 1) * 4) = k0;
              if (fi < 3) *(AS3 f4*)(img + 1152 + ((2 * mt + (fg >> 1)) * 8 + hh * 4 + fi) * 8 + (fg & 1) * 4) = k1;
            } else if (fg == 0) {
              *(AS3 f4*)(img + 1024 + hh * 64 + fi * 4) = k0;
              if (fi < 3) *(AS3 f4*)(img + 1408 + hh * 16 + fi * 4) = k1;
            }
          }
        }
      }
      asm volatile("s_waitcnt lgkmcnt(0)" ::: "memory");
      if (lane == 0) lds_add1(FW(W_KV));
      lds_wait_ge(FW(W_KV), 4);
      {
        const AS3 f4* kp = (const AS3 f4*)(smem + 40 * 1024);
        char* sp = ws + O_SLAB(0) + L * SOBY + (hp * 16 + art) * 3072;
        const int q8 = w * 48 + min(lane, 47), q0 = 2 * q8, q1 = q0 + 1;
        f4 v0 = (kp[q0] + kp[q0 + 384]) + (kp[q0 + 768] + kp[q0 + 1152]);
        f4 v1 = (kp[q1] + kp[q1 + 384]) + (kp[q1 + 768] + kp[q1 + 1152]);
        const bool ok0 = q8 < 144 || (q8 < 176 ? (q8 & 3) != 3 : q8 < 180), ok1 = ok0 && (q8 < 176 || !(q8 & 1));
        if (!ok0) v0 = z4;
        if (!ok1) v1 = z4;
        if (lane < 48) st_wt(sp + q8 * 16, __builtin_bit_cast(u4, cv8(v0, v1)));
        if (lane == 0) lds_add1(FW(W_SL));
      }
      SB();
      STM(3);
      __builtin_amdgcn_s_setprio(0);
#pragma unroll
      for (int s = 0; s < B_E; ++s) { wf[s] = wfl(s); SB(); }
      slab_sum();
      STM(4);
#pragma unroll
      for (int q = 0; q < 3 * M_PD; ++q) UNDEF(wb[q / 3][q % 3]);
    }
    RELANE();
    if (w < 9) {
      const int T = ct * 9 + w;
      const unsigned xo = ((rtb * 2 * 18 + (T >> 1)) * 64 + ((T & 1) * 2 + (fg >> 1)) * 16 + fi) * 16 + (fg & 1) * 8;
      u2 xr[2];
      lds_wait_ge(FW(W_GOB), 8);
#pragma unroll
      for (int q = 0; q < 2; ++q) xr[q] = bld2(rs, xo, L * XBY + q * 18432);
      SB();
      if (w < 8) {
        if (lane == 0) lds_add1(FW(W_BLD));
        lds_wait_ge(gop, 1);
#pragma unroll
        for (int q = 0; q < BM_A; ++q) { wb[q / 3][q % 3] = wld(q / 3, q % 3); SB(); }
        lds_wait_ge(FW(W_BLD), 8);
        asm volatile("s_sleep %0" :: "i"(DLY) : "memory");
        const unsigned oo = O_O(0) + L * OBY + rtb * 36864;
        u4 p[5];
        poll_load<5, SL2>(p, [&](int i) { return i < 4 ? bld4(rs, l16, oo + w * 4096 + i * 1024) : bld4(rs, l16 & 511, oo + 32768 + w * 512); });
#pragma unroll
        for (int i = 0; i < 4; ++i) *(AS3 u4*)(smem + BO + (i & 1) * 18 * 1024 + (w * 2 + (i >> 1)) * 1024 + l16) = p[i];
        const bool hi = lane >= 32;
        AS3 char* tb = (AS3 char*)smem + BO + (16 + (w >> 2)) * 1024 + (w & 3) * 4 + ((lane >> 3) & 1) * 18 * 1024 + ((2 * ((lane >> 4) & 1) + ((lane >> 2) & 1)) * 16 + 4 * (lane & 3) + 2 * (lane >> 5)) * 16;
        *(AS3 unsigned*)tb = hi ? p[4][2] : p[4][0];
        *(AS3 unsigned*)(tb + 16) = hi ? p[4][3] : p[4][1];
        asm volatile("s_waitcnt lgkmcnt(0)" ::: "memory");
        if (lane == 0) lds_add1(FW(W_OR));
      }
      if (L == 1 || w < 4 || w > 7) STM(6);
      lds_wait_ge(FW(W_OR), 8);
      if (L == 1 || w < 4 || w > 7) STM(7);
      f4 acc[2] = {{0, 0, 0, 0}, {0, 0, 0, 0}};
      {
        const AS3 s8* ol = (const AS3 s8*)(smem + BO) + lane;
        s8 ob[3][2];
#pragma unroll
        for (int s = 0; s < 2; ++s) { ob[s][0] = ol[s * 64]; ob[s][1] = ol[(18 + s) * 64]; }
#pragma unroll
        for (int s = 0; s < 18; ++s) {
          if (s + 2 < 18) { ob[(s + 2) % 3][0] = ol[(s + 2) * 64]; ob[(s + 2) % 3][1] = ol[(20 + s) * 64]; }
          SB();
          acc[0] = mfma32(wf[s], ob[s % 3][0], acc[0]);
          acc[1] = mfma32(wf[s], ob[s % 3][1], acc[1]);
          SB();
          if (s % BM_S0 == 0 && BM_A + s / BM_S0 < BM_B) { const int q = BM_A + s / BM_S0; if (w < 8) wb[q / 3][q % 3] = wld(q / 3, q % 3); SB(); }
        }
      }
      AS3 float* red = (AS3 float*)(smem + BRED);
      u2 o[2];
#pragma unroll
      for (int tt = 0; tt < 2; ++tt)
#pragma unroll
        for (int c = 0; c < 2; ++c) o[tt][c] = cvtpk(unpk(cvtpk((f2){acc[tt][2 * c], acc[tt][2 * c + 1]})) + unpk(xr[tt][c]));
      SB();
#pragma unroll
      for (int tt = 0; tt < 2; ++tt) st_wt(ws + O_X1(0) + L * 2 * XBY + xo + tt * 18 * 1024, o[tt]);
      if (lane == 0) lds_add1(FW(W_BDR));
      STM(8);
      if (w < 8) {
#pragma unroll
        for (int q = BM_B; q < BM_PF; ++q) { wb[q / 3][q % 3] = wld(q / 3, q % 3); SB(); }
      }
#pragma unroll
      for (int tt = 0; tt < 2; ++tt) {
        const f2 v[2] = {unpk(o[tt][0]), unpk(o[tt][1])};
        const float m = red_sum((v[0][0] + v[0][1]) + (v[1][0] + v[1][1])) * (1.0f / 16.0f);
        const f2 d0 = v[0] - m, d1 = v[1] - m;
        const float m2 = red_sum((d0[0] * d0[0] + d0[1] * d0[1]) + (d1[0] * d1[0] + d1[1] * d1[1]));
        if (fg == 0) *(AS3 f2*)(red + ((tt * 16 + fi) * 9 + w) * 2) = (f2){m, m2};
      }
      STM(9);
    }
    RELANE();
    int rtt, hf, rks, unit;
#define RIDX() do { int b_ = blockIdx.x, w_ = w0; asm volatile("" : "+s"(b_), "+s"(w_)); rtt = ((b_ >> 2) & 1) * 64 + (b_ >> 4) * 4 + (b_ & 3); hf = (b_ >> 3) & 1; \
      rks = (w_ >> 2) * 6 + hf * 3 + min(w_ & 3, 2); unit = rtt * 18 + rks; } while (0)
    u4 xr, bv;
    const unsigned pbo = O_PART(0) + (slice * 128 + rt * 4) * 18 * 1024 + w * 1536;
    lds_wait_ge(FW(W_BDR), 9);
    asm volatile("s_sleep %0" :: "i"(DLY2) : "memory");
    STM(10);
    {
      const unsigned xs = O_X1(0) + L * 2 * XBY + rt * 72 * 1024 + ((w & 3) * 18 + (w >> 2)) * 1024;
      u4 p[6];
      poll_load<6, SL3>(p, [&](int i) { return bld4(rs, l16, xs + i * 3072); });
#pragma unroll
      for (int i = 0; i < 6; ++i) *(AS3 u4*)(smem + (i * 12 + w) * 1024 + l16) = p[i];
    }
    __builtin_amdgcn_s_setprio(3);
    BARRIER();
    STM(11);
    if (L == 0 && w > 8) {
      if (lds_arrive(FW(W_PD2), lane) == 2 && lane < 8) flag_add((unsigned*)(ws + O_FP2 + (lane * 16 + hp) * 128));
    }
    if (w == 9 && lane < 32) {
      const AS3 float* red = (const AS3 float*)(smem + BRED);
      float mm[9], sm = 0.f, sq = 0.f;
#pragma unroll
      for (int i = 0; i < 9; ++i) { const f2 p = *(const AS3 f2*)(red + (lane * 9 + i) * 2); mm[i] = p[0]; sm += p[0]; sq += p[1]; }
      sm *= (1.0f / 9.0f);
      float dq = 0.f;
#pragma unroll
      for (int i = 0; i < 9; ++i) { const float d = mm[i] - sm; dq += d * d; }
      st_wt(ws + O_ST1(0) + L * 65536 + ((rtb * 32 + lane) * 4 + ct) * 8, __builtin_bit_cast(u2, (f2){sm, sq + 16.f * dq}));
    }
    SB();
    if (w < 8) {
#pragma unroll
      for (int q = BM_PF; q < 3 * M_PD; ++q) { wb[q / 3][q % 3] = wld(q / 3, q % 3); SB(); }
    }
    {
      f4 acc[3][4];
      float r2[4];
      u4 st[2];
      const char* stp = ws + O_ST1(0) + L * 65536 + rt * 2048 + lane * 32;
      const unsigned* f2p = (const unsigned*)(ws + O_FP2 + (xcd * 16 + fi) * 128);
      unsigned f2v;
      const AS3 s8* l0 = (const AS3 s8*)smem + lane;
      const AS3 s8* l1 = opaque_lds(l0 + 4096);
      const AS3 s8* l2 = opaque_lds(l0 + 8192);
      auto lrd = [&](int piece) { return piece < 64 ? l0[piece * 64] : piece < 128 ? l1[(piece - 64) * 64] : l2[(piece - 128) * 64]; };
      SB();
      s8 xf[2][4];
#pragma unroll
      for (int ph = 0; ph < 2; ++ph) {
#pragma unroll
        for (int tt = 0; tt < 4; ++tt) xf[0][tt] = lrd(ph * 72 + tt);
        SB();
#pragma unroll
        for (int ks = 0; ks < 18; ++ks) {
          const int u = ph * 18 + ks;
          if (u == 5) __builtin_amdgcn_s_setprio(2);
          if (u == 10) __builtin_amdgcn_s_setprio(1);
          if (u == 14) __builtin_amdgcn_s_setprio(0);
          if (ph == 1) {
            if (ks == 5) { if (w >= 8) __builtin_amdgcn_s_setprio(2); }
            if (ks == 10) { if (w >= 8) __builtin_amdgcn_s_setprio(1); }
            if (ks == 14) { if (w >= 8) __builtin_amdgcn_s_setprio(0); }
            if (ks == 5 + PR_B) { if (w >= 4 && w < 8) __builtin_amdgcn_s_setprio(2); }
            if (ks == 10 + PR_B) { if (w >= 4 && w < 8) __builtin_amdgcn_s_setprio(1); }
            if (ks == 14 + PR_B) { if (w >= 4 && w < 8) __builtin_amdgcn_s_setprio(0); }
            if (ks == 5 + PR_A) { if (w < 4) __builtin_amdgcn_s_setprio(2); }
            if (ks == 10 + PR_A) { if (w < 4) __builtin_amdgcn_s_setprio(1); }
            if (ks == 14 + PR_A) { if (w < 4) __builtin_amdgcn_s_setprio(0); }
          }
          if (u >= 1 && u < 10 && L == 0) {
            u4 ones;
            asm volatile("v_mov_b32 %0, -1\n\tv_mov_b32 %1, -1\n\tv_mov_b32 %2, -1\n\tv_mov_b32 %3, -1" : "=v"(ones[0]), "=v"(ones[1]), "=v"(ones[2]), "=v"(ones[3]));
            if (u == 9) { RIDX(); if ((w & 3) < 3) __builtin_amdgcn_raw_buffer_store_b128(ones, rs, l16, O_X(1) + unit * 1024, 16); }
            else if (F_B && (w >> 2) == 1) {}
            else if (u & 1) __builtin_amdgcn_raw_buffer_store_b128(ones, rs, l16, pbo + (u >> 1) * 18 * 1024, 16);
            else if (lane < 32) __builtin_amdgcn_raw_buffer_store_b128(ones, rs, l16, pbo + ((u - 1) >> 1) * 18 * 1024 + 1024, 16);
            SB();
          }
          if (u == 10) { asm volatile("global_load_dwordx4 %0, %2, off sc1\n\tglobal_load_dwordx4 %1, %2, off offset:16 sc1" : "=&v"(st[0]), "=&v"(st[1]) : "v"(stp) : "memory"); SB(); }
          if (u == 18 - M_PD - 4) { asm volatile("global_load_dword %0, %1, off sc1" : "=v"(f2v) : "v"(f2p) : "memory"); SB(); }
          if (u == 18 - M_PD) {
            asm volatile("s_waitcnt vmcnt(12)" : "+v"(f2v) :: "memory");
            if (__builtin_amdgcn_ballot_w64(f2v == 16u) != ~0ull) flag_poll(f2p, 16u);
            SB();
          }
          if (u == F_M && L == 0 && w < 9) {
            u4 ones;
            asm volatile("v_mov_b32 %0, -1\n\tv_mov_b32 %1, -1\n\tv_mov_b32 %2, -1\n\tv_mov_b32 %3, -1" : "=v"(ones[0]), "=v"(ones[1]), "=v"(ones[2]), "=v"(ones[3]));
            __builtin_amdgcn_raw_buffer_store_b128(ones, rs, l16, O_O(1) + bid * 18432 + w * 2048, 16);
            __builtin_amdgcn_raw_buffer_store_b128(ones, rs, l16, O_O(1) + bid * 18432 + w * 2048 + 1024, 16);
            SB();
          }
          if (u == 23) { lds_wait_ge(FW(W_H1), 4); SB(); }
          if (u == 29) { lds_wait_ge(FW(W_H2), 4); SB(); }
#pragma unroll
          for (int t = 0; t < 3; ++t) {
#pragma unroll
            for (int tt = 0; tt < 4; ++tt) {
              if (ks == 0) mfma_z(acc[t][tt], wb[u % M_PD][t], xf[ks & 1][tt]);
              else mfma_acc(acc[t][tt], wb[u % M_PD][t], xf[ks & 1][tt]);
              SB();
              if (t == 0 && ks + 1 < 18) { xf[(ks + 1) & 1][tt] = lrd(ph * 72 + (ks + 1) * 4 + tt); SB(); }
            }
            if (u + M_PD < 36) { wb[u % M_PD][t] = wld(u + M_PD, t); SB(); }
          }
        }
        MFMA_FENCE();
        SB();
        if (ph == 0) {
          STM(12);
          if (PR_E) __builtin_amdgcn_s_setprio(3);
          asm volatile("s_waitcnt vmcnt(%2)" : "+v"(st[0]), "+v"(st[1]) : "i"(3 * M_PD) : "memory");
#pragma unroll 1
          for (int it = 0; it < 100000; ++it) {
            const bool bad = st[0][1] == 0xbf800000u || st[0][3] == 0xbf800000u || st[1][1] == 0xbf800000u || st[1][3] == 0xbf800000u;
            if (__builtin_amdgcn_ballot_w64(bad) == 0ull) break;
            asm volatile("global_load_dwordx4 %0, %2, off sc1\n\tglobal_load_dwordx4 %1, %2, off offset:16 sc1\n\ts_waitcnt vmcnt(0)" : "=&v"(st[0]), "=&v"(st[1]) : "v"(stp) : "memory");
          }
          f4 cs[3], bb[3];
#pragma unroll
          for (int t = 0; t < 3; ++t) { const AS3 f4* p = (const AS3 f4*)(smem + M_CB + ((w * 3 + t) * 4 + fg) * 32); cs[t] = p[0]; bb[t] = p[1]; }
          const f4 s0 = __builtin_bit_cast(f4, st[0]), s1 = __builtin_bit_cast(f4, st[1]);
          const float mean = ((s0[0] + s0[2]) + (s1[0] + s1[2])) * 0.25f;
          const float e0 = s0[0] - mean, e1 = s0[2] - mean, e2 = s1[0] - mean, e3 = s1[2] - mean;
          const float var = (((s0[1] + s0[3]) + (s1[1] + s1[3])) + 144.f * ((e0 * e0 + e1 * e1) + (e2 * e2 + e3 * e3))) * (1.0f / 576.0f) + 1e-5f;
          const float rstd = __builtin_amdgcn_rsqf(var), sd = var * rstd, rr = rstd * rstd;
          AS3 char* hb = (AS3 char*)smem + 73728 + ((3 * w) >> 1) * 4096 + lane * 16;
#pragma unroll
          for (int tt = 0; tt < 4; ++tt) {
            const float nm = -bperm(mean, tt * 64 + fi * 4), sg = bperm(sd, tt * 64 + fi * 4);
            r2[tt] = bperm(rr, tt * 64 + fi * 4);
            unsigned pk[3][2];
#pragma unroll
            for (int t = 0; t < 3; ++t)
#pragma unroll
              for (int r = 0; r < 2; ++r) {
                const f2 a2 = {acc[t][tt][2 * r], acc[t][tt][2 * r + 1]}, c2 = {cs[t][2 * r], cs[t][2 * r + 1]}, b2_ = {bb[t][2 * r], bb[t][2 * r + 1]};
                const f2 z = __builtin_elementwise_fma(c2, (f2){nm, nm}, __builtin_elementwise_fma(b2_, (f2){sg, sg}, a2));
                const f2 m = {max2f(z[0], 0.f), max2f(z[1], 0.f)};
                pk[t][r] = cvtpk(m * m);
              }
            if (w & 1) {
              *(AS3 u2*)(hb + tt * 1024 + 8) = (u2){pk[0][0], pk[0][1]};
              *(AS3 u4*)(hb + tt * 1024 + 4096) = (u4){pk[1][0], pk[1][1], pk[2][0], pk[2][1]};
            } else {
              *(AS3 u4*)(hb + tt * 1024) = (u4){pk[0][0], pk[0][1], pk[1][0], pk[1][1]};
              *(AS3 u2*)(hb + tt * 1024 + 4096) = (u2){pk[2][0], pk[2][1]};
            }
          }
          SB();
          asm volatile("s_waitcnt lgkmcnt(0)" ::: "memory");
          if (lane == 0) lds_add1(FW(W_H0) + (w >> 2) * 4);
          __builtin_amdgcn_s_setprio(3);
          lds_wait_ge(FW(W_H0), 4);
          SB();
          STM(13);
        }
      }
      STM(14);
      RIDX();
      __builtin_amdgcn_s_setprio(3);
      if (L == 0 && w == 3 && lane < 24)
        flag_set((unsigned*)(ws + (lane < 16 ? O_FR + ((rtt >> 3) * 16 + lane) * 64 + ((rtt & 7) * 2 + hf) * 4 : O_FM + (rt * 8 + lane - 16) * 192 + 128 + slice * 4)));
      xr = bld4(rs, l16, O_X1(0) + L * 2 * XBY + unit * 1024);
      bv = *(const u4*)(b2 + rks * 32 + fg * 8);
      SB();
      char* pb = ws + O_PART(0) + ((size_t)(slice * 128 + rt * 4) * 18 + ((3 * w) >> 1)) * 1024;
#pragma unroll
      for (int tt = 0; tt < 4; ++tt) {
        unsigned pk[3][2];
#pragma unroll
        for (int t = 0; t < 3; ++t)
#pragma unroll
          for (int r = 0; r < 2; ++r) pk[t][r] = (cvtph((f2){smul(acc[t][tt][2 * r], r2[tt]), smul(acc[t][tt][2 * r + 1], r2[tt])}) & ~1u) | (unsigned)L;
        char* q = pb + tt * 18 * 1024;
        if (w & 1) {
          st_wt(q + 512 + lane * 8, (u2){pk[0][0], pk[0][1]}); st_wt(q + 1024 + lane * 16, (u4){pk[1][0], pk[1][1], pk[2][0], pk[2][1]});
        } else {
          st_wt(q + lane * 16, (u4){pk[0][0], pk[0][1], pk[1][0], pk[1][1]}); st_wt(q + 1024 + lane * 8, (u2){pk[2][0], pk[2][1]});
        }
      }
    }
    STM(15);
    RELANE();
    const char* asrc = ws + O_WQKV + (size_t)hp * 126 * 1024 + (w & 3) * 1024 + l16;
    const unsigned adst = lds_addr(smem) + (w & 3) * 1024;
    if (L == 0 && w < 4) {
      if (w == 3) {
        flag_poll((const unsigned*)(ws + (lane < 16 ? O_FR + (art * 16 + hp) * 64 + fi * 4 : O_FM + (rt * 8 + slice) * 192 + 128 + (lane & 7) * 4)), 1u);
        if (lane == 0) lds_set1(FW(W_GOR));
      }
#pragma unroll 1
      for (int i = 0; i < 18; ++i) glds16s(asrc + i * 4096, adst + i * 4096);
      WAIT_V(18);
    } else WAIT_V(0);
    if (lane == 0) lds_add1(FW(W_MDN));
    if (F_B && L == 0 && (w >> 2) == 1 && lane < 4) flag_set((unsigned*)(ws + O_FM + ((rt * 8 + lane * 2 + ((w >> 1) & 1)) * 3 + 1) * 64 + (slice * 2 + (w & 1)) * 4));
    STM(16);
    if ((w & 3) == 3) {
      if (L == 0 && w == 7 && F_B) {
        flag_poll((const unsigned*)(ws + O_FM + (rt * 8 + slice) * 192 + 64 + fi * 4), 1u);
        if (lane == 0) lds_set1(FW(W_GOR1));
      }
      if (L == 0 && w == 3) {
        WAIT_V(0);
        if (lane == 0) lds_add1(FW(W_WL) + 128);
        STM(18);
      }
    } else {
      if (L == 0) lds_wait_ge(FW(W_GOR) + (F_B && (w >> 2) == 1 ? 4 : 0), 1);
      if (R_DLY) asm volatile("s_sleep %0" :: "i"(R_DLY) : "memory");
      STM(17);
      u4 p[8];
      const unsigned po_ = O_PART(0) + unit * 1024;
      auto bad = [&](u4 v) { const unsigned l = L; return __builtin_amdgcn_ballot_w64((((v[0] ^ l) | (v[1] ^ l) | (v[2] ^ l) | (v[3] ^ l)) & 1u) != 0u) != 0ull; };
      if ((w & 3) == 1) {
        poll_gen<8, SL5>(p, [&](int s) { const u2 a = bld2(rs, lane * 8, po_ + s * XBY), b = bld2(rs, lane * 8, po_ + s * XBY + 512); return (u4){a[0], a[1], b[0], b[1]}; }, bad);
      } else {
        poll_gen<8, SL5>(p, [&](int s) { return bld4(rs, l16, po_ + s * XBY); }, bad);
      }
      u4 o;
      f2 v[4];
      f2 s2 = {0.f, 0.f};
#pragma unroll
      for (int c = 0; c < 4; ++c) {
        f2 a = ((unph(p[0][c]) + unph(p[1][c])) + (unph(p[2][c]) + unph(p[3][c]))) + ((unph(p[4][c]) + unph(p[5][c])) + (unph(p[6][c]) + unph(p[7][c])));
        a = unpk(cvtpk(a)) + unpk(bv[c]);
        a = unpk(cvtpk(a)) + unpk(xr[c]);
        o[c] = cvtpk(a);
        v[c] = unpk(o[c]);
        s2 += v[c];
      }
      STM(18);
      if (L == 1) {
        st_wt(out + (size_t)(rtt * 16 + fi) * D_ + rks * 32 + fg * 8, o);
      } else {
        st_wt(ws + O_X(1) + unit * 1024 + l16, o);
        if (w < 4 && lane == 0) lds_add1(FW(W_WL) + 128);
        AS3 float* red = (AS3 float*)(smem + R_RED);
        const float m = red_sum(s2[0] + s2[1]) * (1.0f / 32.0f);
        f2 q2 = {0.f, 0.f};
#pragma unroll
        for (int c = 0; c < 4; ++c) { const f2 d = v[c] - m; q2 += d * d; }
        const float m2 = red_sum(q2[0] + q2[1]);
        if (fg == 0) *(AS3 f2*)(red + (((w >> 2) * 3 + (w & 3)) * 16 + fi) * 2) = (f2){m, m2};
        asm volatile("s_waitcnt lgkmcnt(0)" ::: "memory");
        if (lds_arrive(FW(W_RRED), lane) == 8 && lane < 16) {
          float mm[9], sm = 0.f, sq = 0.f;
#pragma unroll
          for (int i = 0; i < 9; ++i) { const f2 pr = *(const AS3 f2*)(red + (i * 16 + lane) * 2); mm[i] = pr[0]; sm += pr[0]; sq += pr[1]; }
          sm *= (1.0f / 9.0f);
          float dq = 0.f;
#pragma unroll
          for (int i = 0; i < 9; ++i) { const float d = mm[i] - sm; dq += d * d; }
          st_wt(ws + O_STA1 + ((rtt * 16 + lane) * 2 + hf) * 8, __builtin_bit_cast(u2, (f2){sm, sq + 32.f * dq}));
        }
      }
    }
    if (L == 0 && (w == 7 || w == 11)) {
      lds_wait_ge(FW(W_MDN), 12);
      const int o2 = 69 * 1024 + (w >> 3) * 1024;
#pragma unroll 1
      for (int i = 0; i < 27; ++i) glds16s(asrc + o2 + i * 2048, adst + o2 + i * 2048);
      WAIT_V(0);
      if (lane < 2) lds_add1(FW(W_ADR) + 128);
    }
    __builtin_amdgcn_s_setprio(0);
    STM(19);
  }
  STM_OUT();
  asm volatile("LKEND:");
}

extern "C" void kernel_launch(void* const* d_in, const int* in_sizes, int n_in,
                              void* d_out, int out_size, void* d_ws, size_t ws_size,
                              hipStream_t stream) {
  char* ws = (char*)d_ws;
  kF<<<256, 768, 0, stream>>>(ws, (const bf16*)d_in[0], (const bf16*)d_in[1], (const bf16*)d_in[2], (const bf16*)d_in[3], (const bf16*)d_in[5], (const bf16*)d_in[6],
                              (const bf16*)d_in[7], (const bf16*)d_in[8], (bf16*)d_out);
}
```
